```python
import torch

def _softmax(x, axis=-1):
    return torch.softmax(x, dim=axis)
def _gelu(x):
    return torch.nn.functional.gelu(x, approximate="tanh")
def _silu(x):
    return torch.nn.functional.silu(x)
def _sigmoid(x):
    return torch.sigmoid(x)
def _layernorm(x, axis=-1, eps=1e-5):
    m = x.mean(dim=axis, keepdim=True)
    v = x.var(dim=axis, keepdim=True, unbiased=False)
    return (x - m) / torch.sqrt(v + eps)
def _scatter_add(tgt, idx, src, axis=0):
    return tgt.clone().index_add_(axis, idx, src)
def _segment_sum(src, seg, n):
    out = torch.zeros((n,) + src.shape[1:], dtype=src.dtype)
    return out.index_add_(0, seg, src)
def _pad(x, pw):
    flat = tuple(v for pair in reversed(pw) for v in pair)
    return torch.nn.functional.pad(x, flat)

kernel_name = 'parallel_sum__gqa_block_cached__swiglu_ffn__decode'

def setup_inputs():

    _w = (lambda a: (lambda t: t.to(torch.bfloat16) if t.is_floating_point() else t)(torch.as_tensor(a)))
    def _A_setup():
        B, Sq, D = 1, 1, 576
        Hq, Hkv, Sk, Dh = 32, 16, 8192, 128
        assert Hq % Hkv == 0, (Hq, Hkv)
        return {
            "x": _w((lambda *s: torch.randn(*s, dtype=torch.bfloat16) * 0.05)(B, Sq, D)),
            "k_cache": _w((lambda *s: torch.randn(*s, dtype=torch.bfloat16) * 3.2)(B, Hkv, Sk, Dh)),
            "v_cache": _w((lambda *s: torch.randn(*s, dtype=torch.bfloat16) * 1.6)(B, Hkv, Sk, Dh)),
            "wq": _w((lambda *s: torch.randn(*s, dtype=torch.bfloat16) * 3.2)(D, Hq * Dh) * (D ** -0.5)),
            "wo": _w((lambda *s: torch.randn(*s, dtype=torch.bfloat16) * 1.6)(Hq * Dh, D) * ((Hq * Dh) ** -0.5)),
        }
    _A_in = _A_setup()

    def _B_setup():
        B, S, D, F = 1, 1, 576, 4608
        return {
            "x": _w((lambda *s: torch.randn(*s, dtype=torch.bfloat16) * 0.05)(B, S, D)),
            "w_gate": _w((lambda *s: torch.randn(*s, dtype=torch.bfloat16) * 0.05)(D, F)),
            "w_up": _w((lambda *s: torch.randn(*s, dtype=torch.bfloat16) * 0.05)(D, F)),
            "w_down": _w((lambda *s: torch.randn(*s, dtype=torch.bfloat16) * 0.05)(F, D)),
        }
    _B_in = _B_setup()

    return {
        "A_x": _A_in["x"],
        "A_k_cache": _A_in["k_cache"],
        "A_v_cache": _A_in["v_cache"],
        "A_wq": _A_in["wq"],
        "A_wo": _A_in["wo"],
        "B_x": _B_in["x"],
        "B_w_gate": _B_in["w_gate"],
        "B_w_up": _B_in["w_up"],
        "B_w_down": _B_in["w_down"],
    }

def reference(A_x, A_k_cache, A_v_cache, A_wq, A_wo, B_x, B_w_gate, B_w_up, B_w_down):
    def _A_ref(x, k_cache, v_cache, wq, wo):
        B, Sq, D = x.shape
        Hq, Dh = 32, 128
        Hkv = k_cache.shape[1]
        rep = Hq // Hkv
        q = (lambda x, *d: x.permute(*d))((x @ wq).reshape(B, Sq, Hq, Dh), 0, 2, 1, 3)
        k = (lambda x, n, axis: torch.repeat_interleave(x, n, dim=axis))(k_cache, rep, axis=1)
        v = (lambda x, n, axis: torch.repeat_interleave(x, n, dim=axis))(v_cache, rep, axis=1)
        scores = torch.einsum("bhqd,bhkd->bhqk", q, k) * (Dh ** -0.5)
        p = _softmax(scores, axis=-1)
        o = torch.einsum("bhqk,bhkd->bhqd", p, v)
        return (lambda x, *d: x.permute(*d))(o, 0, 2, 1, 3).reshape(B, Sq, Hq * Dh) @ wo

    def _B_ref(x, w_gate, w_up, w_down):
        g = _silu(x @ w_gate)
        u = x @ w_up
        return (g * u) @ w_down

    x = A_x
    xn = _layernorm(x, axis=-1)
    return x + _A_ref(xn, A_k_cache, A_v_cache, A_wq, A_wo) + _B_ref(xn, B_w_gate, B_w_up, B_w_down)
```

```cpp
#include <hip/hip_runtime.h>
#include <hip/hip_bf16.h>
#include <cstdint>

typedef unsigned u32x4 __attribute__((ext_vector_type(4)));
typedef float f32x4 __attribute__((ext_vector_type(4)));
typedef __attribute__((ext_vector_type(2))) __bf16 bf16x2;

constexpr int D = 576, HQ = 32, HKV = 16, SK = 8192, DH = 128, F = 4608;
constexpr int NQ = HQ * DH;

__device__ __forceinline__ float bflo(unsigned u) { return __uint_as_float(u << 16); }
__device__ __forceinline__ float bfhi(unsigned u) { return __uint_as_float(u & 0xffff0000u); }
__device__ __forceinline__ float bfround(float x) { return __bfloat162float(__float2bfloat16(x)); }

__device__ void layernorm_to_lds(const unsigned short* x, float* xn, float* red) {
  int t = threadIdx.x;
  float s = 0.f;
  for (int i = t; i < D; i += blockDim.x) s += __uint_as_float((unsigned)x[i] << 16);
  for (int o = 32; o; o >>= 1) s += __shfl_xor(s, o);
  if ((t & 63) == 0) red[t >> 6] = s;
  __syncthreads();
  float tot = 0.f;
  for (int w = 0; w < (int)(blockDim.x >> 6); ++w) tot += red[w];
  float m = tot / D;
  __syncthreads();
  float v = 0.f;
  for (int i = t; i < D; i += blockDim.x) { float d = __uint_as_float((unsigned)x[i] << 16) - m; v += d * d; }
  for (int o = 32; o; o >>= 1) v += __shfl_xor(v, o);
  if ((t & 63) == 0) red[t >> 6] = v;
  __syncthreads();
  tot = 0.f;
  for (int w = 0; w < (int)(blockDim.x >> 6); ++w) tot += red[w];
  float var = tot / D;
  float rs = 1.0f / sqrtf(var + 1e-5f);
  for (int i = t; i < D; i += blockDim.x) xn[i] = bfround((__uint_as_float((unsigned)x[i] << 16) - m) * rs);
  __syncthreads();
}

__global__ void __launch_bounds__(256) k_proj(const unsigned short* x, const unsigned short* wq, const unsigned short* wg, const unsigned short* wu,
                                              float* q, float* g, float* u) {
  __shared__ float xn[D];
  __shared__ float red[8];
  __shared__ float part[32][64];
  layernorm_to_lds(x, xn, red);
  int b = blockIdx.x, t = threadIdx.x;
  const unsigned short* W; int N; float* out; int c0;
  if (b < 64) { W = wq; N = NQ; out = q; c0 = b * 64; }
  else if (b < 64 + 72) { W = wg; N = F; out = g; c0 = (b - 64) * 64; }
  else { W = wu; N = F; out = u; c0 = (b - 136) * 64; }
  int cg = t & 7, rg = t >> 3;
  float acc[8] = {0, 0, 0, 0, 0, 0, 0, 0};
#pragma unroll
  for (int i = 0; i < 18; ++i) {
    int r = rg + 32 * i;
    u32x4 w = *(const u32x4*)(W + (size_t)r * N + c0 + cg * 8);
    float xv = xn[r];
#pragma unroll
    for (int k = 0; k < 4; ++k) { acc[2 * k] += xv * bflo(w[k]); acc[2 * k + 1] += xv * bfhi(w[k]); }
  }
#pragma unroll
  for (int k = 0; k < 8; ++k) part[rg][cg * 8 + k] = acc[k];
  __syncthreads();
  if (t < 64) { float s = 0.f; for (int i = 0; i < 32; ++i) s += part[i][t]; out[c0 + t] = s; }
}

__global__ void __launch_bounds__(256) k_attn(const unsigned short* K, const unsigned short* V, const float* q, float* po, float* pml) {
  __shared__ float sc[2][512];
  __shared__ float red[8];
  __shared__ float oacc[16][2][128];
  int b = blockIdx.x, t = threadIdx.x;
  int kvh = b >> 4, sp = b & 15;
  int c = t & 15, r = t >> 4;
  float qv[2][8];
  for (int h = 0; h < 2; ++h) for (int k = 0; k < 8; ++k) qv[h][k] = bfround(q[(kvh * 2 + h) * DH + c * 8 + k]);
  const unsigned short* Kb = K + ((size_t)kvh * SK + sp * 512) * DH;
  const unsigned short* Vb = V + ((size_t)kvh * SK + sp * 512) * DH;
  for (int i = 0; i < 32; ++i) {
    int key = r + 16 * i;
    u32x4 kk = *(const u32x4*)(Kb + (size_t)key * DH + c * 8);
    float s0 = 0, s1 = 0;
    for (int k = 0; k < 4; ++k) { s0 += qv[0][2 * k] * bflo(kk[k]) + qv[0][2 * k + 1] * bfhi(kk[k]); s1 += qv[1][2 * k] * bflo(kk[k]) + qv[1][2 * k + 1] * bfhi(kk[k]); }
    for (int o = 8; o; o >>= 1) { s0 += __shfl_xor(s0, o); s1 += __shfl_xor(s1, o); }
    if (c == 0) { sc[0][key] = s0 * 0.08838834764831845f; sc[1][key] = s1 * 0.08838834764831845f; }
  }
  __syncthreads();
  float M[2], L[2];
  for (int h = 0; h < 2; ++h) {
    float m = fmaxf(sc[h][t], sc[h][t + 256]);
    for (int o = 32; o; o >>= 1) m = fmaxf(m, __shfl_xor(m, o));
    if ((t & 63) == 0) red[t >> 6] = m;
    __syncthreads();
    m = fmaxf(fmaxf(red[0], red[1]), fmaxf(red[2], red[3]));
    __syncthreads();
    float p0 = __expf(sc[h][t] - m), p1 = __expf(sc[h][t + 256] - m);
    sc[h][t] = p0; sc[h][t + 256] = p1;
    float l = p0 + p1;
    for (int o = 32; o; o >>= 1) l += __shfl_xor(l, o);
    if ((t & 63) == 0) red[t >> 6] = l;
    __syncthreads();
    l = red[0] + red[1] + red[2] + red[3];
    __syncthreads();
    M[h] = m; L[h] = l;
  }
  float a0[8] = {0}, a1[8] = {0};
  for (int i = 0; i < 32; ++i) {
    int key = r + 16 * i;
    u32x4 vv = *(const u32x4*)(Vb + (size_t)key * DH + c * 8);
    float p0 = sc[0][key], p1 = sc[1][key];
    for (int k = 0; k < 4; ++k) { a0[2 * k] += p0 * bflo(vv[k]); a0[2 * k + 1] += p0 * bfhi(vv[k]); a1[2 * k] += p1 * bflo(vv[k]); a1[2 * k + 1] += p1 * bfhi(vv[k]); }
  }
  for (int k = 0; k < 8; ++k) { oacc[r][0][c * 8 + k] = a0[k]; oacc[r][1][c * 8 + k] = a1[k]; }
  __syncthreads();
  {
    int h = t >> 7, d = t & 127;
    float s = 0.f;
    for (int i = 0; i < 16; ++i) s += oacc[i][h][d];
    po[((size_t)(kvh * 2 + h) * 16 + sp) * DH + d] = s;
    if (d == 0) { pml[((kvh * 2 + h) * 16 + sp) * 2] = M[h]; pml[((kvh * 2 + h) * 16 + sp) * 2 + 1] = L[h]; }
  }
}

__global__ void __launch_bounds__(576) k_out(const unsigned short* wo, const unsigned short* wd, const float* po, const float* pml,
                                             const float* g, const float* u, float* part) {
  __shared__ float a[34];
  int b = blockIdx.x, t = threadIdx.x;
  if (t < 16) {
    int row = b * 16 + t; int h = row >> 7, d = row & 127;
    float M = -INFINITY;
    for (int s = 0; s < 16; ++s) M = fmaxf(M, pml[(h * 16 + s) * 2]);
    float L = 0.f, o = 0.f;
    for (int s = 0; s < 16; ++s) { float w = __expf(pml[(h * 16 + s) * 2] - M); L += w * pml[(h * 16 + s) * 2 + 1]; o += w * po[((size_t)h * 16 + s) * DH + d]; }
    a[t] = o / L;
  } else if (t < 34) {
    int j = b * 18 + (t - 16);
    float gv = g[j], uv = u[j];
    a[t] = gv / (1.f + __expf(-gv)) * uv;
  }
  __syncthreads();
  float s = 0.f;
  for (int i = 0; i < 16; ++i) s += a[i] * __uint_as_float((unsigned)wo[(size_t)(b * 16 + i) * D + t] << 16);
  for (int i = 0; i < 18; ++i) s += a[16 + i] * __uint_as_float((unsigned)wd[(size_t)(b * 18 + i) * D + t] << 16);
  part[(size_t)b * D + t] = s;
}

__global__ void __launch_bounds__(256) k_final(const unsigned short* x, const float* part, __hip_bfloat16* out) {
  __shared__ float red[4];
  int c = blockIdx.x, t = threadIdx.x;
  float s = part[(size_t)t * D + c];
  for (int o = 32; o; o >>= 1) s += __shfl_xor(s, o);
  if ((t & 63) == 0) red[t >> 6] = s;
  __syncthreads();
  if (t == 0) out[c] = __float2bfloat16(__uint_as_float((unsigned)x[c] << 16) + red[0] + red[1] + red[2] + red[3]);
}

extern "C" void kernel_launch(void* const* d_in, const int* in_sizes, int n_in, void* d_out, int out_size, void* d_ws, size_t ws_size,
                              hipStream_t stream) {
  const unsigned short* x = (const unsigned short*)d_in[0];
  const unsigned short* K = (const unsigned short*)d_in[1];
  const unsigned short* V = (const unsigned short*)d_in[2];
  const unsigned short* wq = (const unsigned short*)d_in[3];
  const unsigned short* wo = (const unsigned short*)d_in[4];
  const unsigned short* wg = (const unsigned short*)d_in[6];
  const unsigned short* wu = (const unsigned short*)d_in[7];
  const unsigned short* wd = (const unsigned short*)d_in[8];
  float* ws = (float*)d_ws;
  float* q = ws; float* g = ws + 4096; float* u = g + 4608; float* po = ws + 16384; float* pml = po + 32 * 16 * 128; float* part = pml + 1024;
  k_proj<<<208, 256, 0, stream>>>(x, wq, wg, wu, q, g, u);
  k_attn<<<256, 256, 0, stream>>>(K, V, q, po, pml);
  k_out<<<256, 576, 0, stream>>>(wo, wd, po, pml, g, u, part);
  k_final<<<576, 256, 0, stream>>>(x, part, (__hip_bfloat16*)d_out);
}
```

```cpp
#include <hip/hip_runtime.h>
#include <hip/hip_bf16.h>
#include <cstdint>

typedef unsigned u32;
typedef u32 u32x4 __attribute__((ext_vector_type(4)));
typedef u32 u32x2 __attribute__((ext_vector_type(2)));
typedef float f32x4 __attribute__((ext_vector_type(4)));
typedef float f32x2 __attribute__((ext_vector_type(2)));
typedef __bf16 bf16x2 __attribute__((ext_vector_type(2)));
typedef __amdgpu_buffer_rsrc_t rsrc_t;

#define WIN 3

constexpr u32 WS_E = 0;
constexpr u32 WS_QX = 4096;
constexpr u32 WS_HX = WS_QX + 256 * 512;
constexpr u32 WS_OX = WS_HX + 256 * 1152;
constexpr u32 WS_FX = WS_OX + 256 * 16 * 144;
constexpr u32 WS_END = WS_FX + 144 * 256 * 32;
constexpr u32 SPIN_G = 40000;
constexpr u32 SPIN_L = 4000000;
constexpr u32 SENT = 0xFFFFFFFFu;

#define STAMP(i) do { } while (0)

#define AUX_NT 2
#define AUX_SC1 16
#define FENCE() __builtin_amdgcn_sched_barrier(0)
#define THROTTLE() do { asm volatile("s_waitcnt vmcnt(%0)" :: "n"(WIN - 1) : "memory"); FENCE(); } while (0)
#define LDSWAIT() asm volatile("s_waitcnt lgkmcnt(0)" ::: "memory")

__device__ __forceinline__ float bflo(u32 u) { return __uint_as_float(u << 16); }
__device__ __forceinline__ float bfhi(u32 u) { return __uint_as_float(u & 0xffff0000u); }
__device__ __forceinline__ float dot2(u32 a, u32 b, float c) {
  return __builtin_amdgcn_fdot2_f32_bf16(__builtin_bit_cast(bf16x2, a), __builtin_bit_cast(bf16x2, b), c, false);
}
template <int CTRL> __device__ __forceinline__ float dpp(float x) {
  return __int_as_float(__builtin_amdgcn_update_dpp(0, __float_as_int(x), CTRL, 0xf, 0xf, true));
}
__device__ __forceinline__ float red32(float a, float b) {
  auto r = __builtin_amdgcn_permlane32_swap(__float_as_uint(a), __float_as_uint(b), false, false);
  return __uint_as_float(r[0]) + __uint_as_float(r[1]);
}
__device__ __forceinline__ float red16(float a, float b) {
  auto r = __builtin_amdgcn_permlane16_swap(__float_as_uint(a), __float_as_uint(b), false, false);
  return __uint_as_float(r[0]) + __uint_as_float(r[1]);
}
__device__ __forceinline__ float max32(float a) {
  auto r = __builtin_amdgcn_permlane32_swap(__float_as_uint(a), __float_as_uint(a), false, false);
  return fmaxf(__uint_as_float(r[0]), __uint_as_float(r[1]));
}
__device__ __forceinline__ float max16(float a) {
  auto r = __builtin_amdgcn_permlane16_swap(__float_as_uint(a), __float_as_uint(a), false, false);
  return fmaxf(__uint_as_float(r[0]), __uint_as_float(r[1]));
}
__device__ __forceinline__ float rowsum16(float s) {
  s += dpp<0xB1>(s); s += dpp<0x4E>(s); s += dpp<0x141>(s); s += dpp<0x140>(s);
  return s;
}
__device__ __forceinline__ float wavesum(float s) { s = rowsum16(s); s = red16(s, s); s = red32(s, s); return s; }
__device__ __forceinline__ rsrc_t mkr(const void* p, u32 n) { return __builtin_amdgcn_make_buffer_rsrc((void*)p, 0, n, 0x00020000); }
__device__ __forceinline__ u32x4 gran(float a, float b, u32 tag) { u32x4 r = {__float_as_uint(a), tag, __float_as_uint(b), tag}; return r; }
__device__ __forceinline__ bool tagok(u32x4 v, u32 tag) { return v[1] == tag && v[3] == tag; }
__device__ __forceinline__ u32 lds_ld(volatile u32* p) { return *p; }

struct __align__(16) Smem {
  float qp[16][64];
  float hp[36][144];
  float acc[16][256];
  float lsum[2][16];
  float wmax[2][16];
  u32 qv[2][2][64];
  u32 hv[24];
  u32 ov[16];
  u32 cq, ch, cm, ca, fq, pad0, pad1, pad2;
};

__global__ void __launch_bounds__(1024, 4)
fused_decode(const unsigned short* __restrict__ x, const unsigned short* __restrict__ Kc, const unsigned short* __restrict__ Vc,
             const unsigned short* __restrict__ wq, const unsigned short* __restrict__ wo, const unsigned short* __restrict__ wg,
             const unsigned short* __restrict__ wu, const unsigned short* __restrict__ wd, unsigned char* __restrict__ ws,
             unsigned short* __restrict__ out) {
  __shared__ Smem sm;
  const int b = blockIdx.x;
  const int w = __builtin_amdgcn_readfirstlane(threadIdx.x >> 6);
  const int l = threadIdx.x & 63;
  const int u = (b & 7) * 32 + (b >> 3);
  const int g = u >> 4, j = u & 15, k4 = u & 3;
  const rsrc_t rX = mkr(x, 1152), rK = mkr(Kc, 33554432u), rV = mkr(Vc, 33554432u), rWS = mkr(ws, WS_END);
  const int r0 = 144 * k4;

  const u32x4 xa = __builtin_amdgcn_raw_buffer_load_b128(rX, l * 16, 0, 0);
  const u32 xb = __builtin_amdgcn_raw_buffer_load_b32(rX, 1024 + l * 4, 0, 0);
  const u32 Ev = __builtin_amdgcn_raw_buffer_load_b32(rWS, WS_E, 0, AUX_SC1);
  const int rq = r0 + 8 * w + (l >> 3);
  const u32 xq0 = __builtin_amdgcn_raw_buffer_load_b16(rX, rq * 2, 0, 0);
  const rsrc_t rQ = mkr(wq, 576u * 8192u);
  const u32 qcol = (u32)(256 * g + 64 * (j >> 2)) * 2 + (l & 7) * 16;
  const u32 kvoff = (u32)u * 131072u + (u32)w * 8192u + (u32)l * 16u;

  if (threadIdx.x < 48) {
    u32* z = sm.hv;
    z[l] = l < 40 ? SENT : 0u;
  }

  u32x4 kk[8], vv[8];
  float mean, rstd;
  u32 tag;
#define LN_STATS() do {                                                                                   \
    float s_ = 0.f;                                                                                       \
    _Pragma("unroll") for (int e = 0; e < 4; ++e) s_ += bflo(xa[e]) + bfhi(xa[e]);                        \
    s_ += bflo(xb) + bfhi(xb);                                                                            \
    mean = wavesum(s_) * (1.0f / 576.0f);                                                                 \
    float v_ = 0.f;                                                                                       \
    _Pragma("unroll") for (int e = 0; e < 4; ++e) { float d0 = bflo(xa[e]) - mean, d1 = bfhi(xa[e]) - mean; v_ += d0 * d0 + d1 * d1; } \
    { float d0 = bflo(xb) - mean, d1 = bfhi(xb) - mean; v_ += l < 32 ? d0 * d0 + d1 * d1 : 0.f; }         \
    rstd = 1.0f / sqrtf(wavesum(v_) * (1.0f / 576.0f) + 1e-5f);                                           \
    tag = __builtin_amdgcn_readfirstlane(Ev) * 0x9E3779B1u + 0x7F4A7C15u;                                 \
  } while (0)
#define QPART(P) do {                                                                                     \
    float a4[4], a2[2];                                                                                   \
    _Pragma("unroll") for (int e = 0; e < 4; ++e) a4[e] = red32(P[e], P[e + 4]);                          \
    _Pragma("unroll") for (int e = 0; e < 2; ++e) a2[e] = red16(a4[e], a4[e + 2]);                        \
    float t_ = (l & 8) ? a2[0] : a2[1], kp_ = (l & 8) ? a2[1] : a2[0];                                    \
    float a1 = kp_ + dpp<0x128>(t_);                                                                      \
    sm.qp[w][8 * (l & 7) + 4 * (l >> 5) + 2 * ((l >> 4) & 1) + ((l >> 3) & 1)] = a1;                      \
    LDSWAIT();                                                                                            \
    if (l == 0) __hip_atomic_fetch_add(&sm.cq, 1u, __ATOMIC_RELAXED, __HIP_MEMORY_SCOPE_WORKGROUP);       \
  } while (0)

  if (w < 12) {
    const u32x4 q0 = __builtin_amdgcn_raw_buffer_load_b128(rQ, (u32)rq * 8192u + qcol, 0, AUX_NT);
    const int slot = l / 9, c9 = l - slot * 9;
    const int s3 = slot >= 3 ? slot - 3 : slot;
    const int rg = r0 + 12 * w + s3;
    const bool gact = l < 54;
    const unsigned char* gp = (const unsigned char*)(slot >= 3 ? wu : wg) + (size_t)(gact ? rg : 0) * 9216 + (u >> 2) * 144 + c9 * 16;
    u32 xg[4];
#pragma unroll
    for (int i = 0; i < 4; ++i) xg[i] = __builtin_amdgcn_raw_buffer_load_b16(rX, (rg + 3 * i) * 2, 0, 0);
    u32x4 gu[4];
    FENCE();
    gu[0] = __builtin_nontemporal_load((const u32x4*)gp); FENCE();
    gu[1] = __builtin_nontemporal_load((const u32x4*)(gp + 3 * 9216)); FENCE();
    LDSWAIT(); __builtin_amdgcn_s_barrier(); FENCE();
    LN_STATS();
    THROTTLE();
    gu[2] = __builtin_nontemporal_load((const u32x4*)(gp + 6 * 9216)); FENCE();
    {
      const float xn = (bflo(xq0) - mean) * rstd;
      float p[8];
#pragma unroll
      for (int e = 0; e < 4; ++e) { p[2 * e] = xn * bflo(q0[e]); p[2 * e + 1] = xn * bfhi(q0[e]); }
      QPART(p);
    }
    if (w == 0) STAMP(1);
    THROTTLE();
    gu[3] = __builtin_nontemporal_load((const u32x4*)(gp + 9 * 9216)); FENCE();
    float ga[8];
#pragma unroll
    for (int e = 0; e < 8; ++e) ga[e] = 0.f;
#pragma unroll
    for (int i = 0; i < 4; ++i) {
      if (i) { THROTTLE(); kk[i - 1] = __builtin_amdgcn_raw_buffer_load_b128(rK, kvoff + (i - 1) * 1024, 0, AUX_NT); FENCE(); }
      const float xn = (bflo(xg[i]) - mean) * rstd;
#pragma unroll
      for (int e = 0; e < 4; ++e) { ga[2 * e] += xn * bflo(gu[i][e]); ga[2 * e + 1] += xn * bfhi(gu[i][e]); }
    }
    if (gact) {
      float* hp = &sm.hp[w * 3 + s3][(slot >= 3 ? 72 : 0) + 8 * c9];
      *(f32x4*)hp = f32x4{ga[0], ga[1], ga[2], ga[3]};
      *(f32x4*)(hp + 4) = f32x4{ga[4], ga[5], ga[6], ga[7]};
    }
    LDSWAIT();
    if (l == 0) __hip_atomic_fetch_add(&sm.ch, 1u, __ATOMIC_RELAXED, __HIP_MEMORY_SCOPE_WORKGROUP);
    if (w == 0) STAMP(2);
    FENCE();
  } else {
    const u32x4 q0 = __builtin_amdgcn_raw_buffer_load_b128(rQ, (u32)rq * 8192u + qcol, 0, AUX_NT);
    const int rq1 = r0 + 8 * (w + 4) + (l >> 3);
    const u32 xq1 = __builtin_amdgcn_raw_buffer_load_b16(rX, rq1 * 2, 0, 0);
    const u32x4 q1 = __builtin_amdgcn_raw_buffer_load_b128(rQ, w < 14 ? (u32)rq1 * 8192u + qcol : 0xFFFFFF00u, 0, AUX_NT);
    FENCE();
    kk[0] = __builtin_amdgcn_raw_buffer_load_b128(rK, kvoff, 0, AUX_NT); FENCE();
    LDSWAIT(); __builtin_amdgcn_s_barrier(); FENCE();
    LN_STATS();
    THROTTLE();
    kk[1] = __builtin_amdgcn_raw_buffer_load_b128(rK, kvoff + 1024, 0, AUX_NT); FENCE();
    THROTTLE();
    kk[2] = __builtin_amdgcn_raw_buffer_load_b128(rK, kvoff + 2048, 0, AUX_NT); FENCE();
    {
      const float xn0 = (bflo(xq0) - mean) * rstd;
      const float xn1 = w < 14 ? (bflo(xq1) - mean) * rstd : 0.f;
      float p[8];
#pragma unroll
      for (int e = 0; e < 4; ++e) { p[2 * e] = xn0 * bflo(q0[e]) + xn1 * bflo(q1[e]); p[2 * e + 1] = xn0 * bfhi(q0[e]) + xn1 * bfhi(q1[e]); }
      QPART(p);
    }
    if (w == 12) {
      for (u32 it = 0; lds_ld(&sm.cq) != 16u && it < SPIN_L; ++it) __builtin_amdgcn_s_sleep(1);
      asm volatile("" ::: "memory");
      const int P = l & 31, hf = l >> 5;
      f32x2 s = {0.f, 0.f};
#pragma unroll
      for (int i = 0; i < 8; ++i) s += *(const f32x2*)&sm.qp[hf * 8 + i][2 * P];
      const float s0 = red32(s[0], s[0]), s1 = red32(s[1], s[1]);
      if (l < 32) __builtin_amdgcn_raw_buffer_store_b128(gran(s0, s1, tag), rWS, WS_QX + (u32)u * 512u + P * 16, 0, AUX_SC1);
      STAMP(3);
    }
    FENCE();
  }

  THROTTLE(); kk[3] = __builtin_amdgcn_raw_buffer_load_b128(rK, kvoff + 3072, 0, AUX_NT); FENCE();
  THROTTLE(); kk[4] = __builtin_amdgcn_raw_buffer_load_b128(rK, kvoff, 4096, AUX_NT); FENCE();
  if (w == 13) {
    STAMP(4);
    u32x4 t[8];
    const u32 qb = WS_QX + (u32)g * 8192u + (u32)(l >> 5) * 2048u + (l & 31) * 16;
    for (u32 it = 0;; ++it) {
      asm volatile("" ::: "memory");
      bool ok = true;
#pragma unroll
      for (int i = 0; i < 8; ++i) t[i] = __builtin_amdgcn_raw_buffer_load_b128(rWS, qb + (i & 3) * 512, (i >> 2) * 4096, AUX_SC1);
#pragma unroll
      for (int i = 0; i < 8; ++i) ok &= tagok(t[i], tag);
      if (__all(ok) || it >= SPIN_G) break;
    }
    const float qs = 0.08838834764831845f * 1.4426950408889634f;
#pragma unroll
    for (int h = 0; h < 2; ++h) {
      float a = ((__uint_as_float(t[4 * h][0]) + __uint_as_float(t[4 * h + 1][0])) + __uint_as_float(t[4 * h + 2][0])) + __uint_as_float(t[4 * h + 3][0]);
      float c = ((__uint_as_float(t[4 * h][2]) + __uint_as_float(t[4 * h + 1][2])) + __uint_as_float(t[4 * h + 2][2])) + __uint_as_float(t[4 * h + 3][2]);
      a *= qs; c *= qs;
      const bf16x2 hi = {(__bf16)a, (__bf16)c};
      const u32 hib = __builtin_bit_cast(u32, hi);
      const bf16x2 lo = {(__bf16)(a - bflo(hib)), (__bf16)(c - bfhi(hib))};
      sm.qv[h][0][l] = hib;
      sm.qv[h][1][l] = __builtin_bit_cast(u32, lo);
    }
    LDSWAIT();
    if (l == 0) *(volatile u32*)&sm.fq = 1u;
    STAMP(5);
    FENCE();
  }
  THROTTLE(); kk[5] = __builtin_amdgcn_raw_buffer_load_b128(rK, kvoff + 1024, 4096, AUX_NT); FENCE();
  THROTTLE(); kk[6] = __builtin_amdgcn_raw_buffer_load_b128(rK, kvoff + 2048, 4096, AUX_NT); FENCE();
  THROTTLE(); kk[7] = __builtin_amdgcn_raw_buffer_load_b128(rK, kvoff + 3072, 4096, AUX_NT); FENCE();
  if (w >= 12) {
    for (u32 it = 0; lds_ld(&sm.ch) != 12u && it < SPIN_L; ++it) __builtin_amdgcn_s_sleep(1);
    asm volatile("" ::: "memory");
    const int v = (l & 31) < 18 ? (l & 31) : 0, hf = l >> 5, cw = w - 12;
    float sg = 0.f, su = 0.f;
#pragma unroll
    for (int i = 0; i < 18; ++i) { sg += sm.hp[hf * 18 + i][18 * cw + v]; su += sm.hp[hf * 18 + i][72 + 18 * cw + v]; }
    sg = red32(sg, sg); su = red32(su, su);
    if (l < 18) __builtin_amdgcn_raw_buffer_store_b128(gran(sg, su, tag), rWS, WS_HX + (u32)((u >> 2) * 4 + cw) * 1152u + k4 * 288 + l * 16, 0, AUX_SC1);
    if (w == 12) STAMP(6);
    FENCE();
  }
  THROTTLE(); vv[0] = __builtin_amdgcn_raw_buffer_load_b128(rV, kvoff, 0, AUX_NT); FENCE();
  THROTTLE(); vv[1] = __builtin_amdgcn_raw_buffer_load_b128(rV, kvoff + 1024, 0, AUX_NT); FENCE();
  THROTTLE(); vv[2] = __builtin_amdgcn_raw_buffer_load_b128(rV, kvoff + 2048, 0, AUX_NT); FENCE();
  if (w == 0) STAMP(7);
  for (u32 it = 0; lds_ld(&sm.fq) == 0u && it < SPIN_L; ++it) __builtin_amdgcn_s_sleep(1);
  asm volatile("" ::: "memory");
  if (w == 0) STAMP(8);
  float s[8][2];
  {
    const int c = l & 15;
    const u32x4 qh0 = *(const u32x4*)&sm.qv[0][0][4 * c], ql0 = *(const u32x4*)&sm.qv[0][1][4 * c];
    const u32x4 qh1 = *(const u32x4*)&sm.qv[1][0][4 * c], ql1 = *(const u32x4*)&sm.qv[1][1][4 * c];
#pragma unroll
    for (int i = 0; i < 8; ++i) {
      if (i == 4) { THROTTLE(); vv[3] = __builtin_amdgcn_raw_buffer_load_b128(rV, kvoff + 3072, 0, AUX_NT); FENCE(); }
      float a0 = 0.f, a1 = 0.f;
#pragma unroll
      for (int e = 0; e < 4; ++e) { a0 = dot2(kk[i][e], ql0[e], a0); a1 = dot2(kk[i][e], ql1[e], a1); }
#pragma unroll
      for (int e = 0; e < 4; ++e) { a0 = dot2(kk[i][e], qh0[e], a0); a1 = dot2(kk[i][e], qh1[e], a1); }
      s[i][0] = rowsum16(a0); s[i][1] = rowsum16(a1);
    }
  }
  float m[2], ls[2];
#pragma unroll
  for (int h = 0; h < 2; ++h) {
    float mm = fmaxf(fmaxf(fmaxf(s[0][h], s[1][h]), fmaxf(s[2][h], s[3][h])), fmaxf(fmaxf(s[4][h], s[5][h]), fmaxf(s[6][h], s[7][h])));
    mm = max32(max16(mm));
    m[h] = mm;
    float t = 0.f;
#pragma unroll
    for (int i = 0; i < 8; ++i) { s[i][h] = __builtin_amdgcn_exp2f(s[i][h] - mm); t += s[i][h]; }
    ls[h] = t;
  }
  if (l == 0) { sm.wmax[0][w] = m[0]; sm.wmax[1][w] = m[1]; }
  LDSWAIT();
  if (l == 0) __hip_atomic_fetch_add(&sm.cm, 1u, __ATOMIC_RELAXED, __HIP_MEMORY_SCOPE_WORKGROUP);
  if (w == 0) STAMP(9);
  FENCE();
  float acc[2][8];
#pragma unroll
  for (int e = 0; e < 8; ++e) { acc[0][e] = 0.f; acc[1][e] = 0.f; }
#define PV(i) do {                                                                                                    \
    _Pragma("unroll") for (int e = 0; e < 4; ++e) {                                                                   \
      const float v0 = bflo(vv[i][e]), v1 = bfhi(vv[i][e]);                                                           \
      acc[0][2 * e] += s[i][0] * v0; acc[0][2 * e + 1] += s[i][0] * v1;                                               \
      acc[1][2 * e] += s[i][1] * v0; acc[1][2 * e + 1] += s[i][1] * v1; }                                             \
    FENCE(); } while (0)
  THROTTLE(); vv[4] = __builtin_amdgcn_raw_buffer_load_b128(rV, kvoff, 4096, AUX_NT); FENCE();
  PV(0); PV(1);
  THROTTLE(); vv[5] = __builtin_amdgcn_raw_buffer_load_b128(rV, kvoff + 1024, 4096, AUX_NT); FENCE();
  PV(2);
  THROTTLE(); vv[6] = __builtin_amdgcn_raw_buffer_load_b128(rV, kvoff + 2048, 4096, AUX_NT); FENCE();
  PV(3);
  THROTTLE(); vv[7] = __builtin_amdgcn_raw_buffer_load_b128(rV, kvoff + 3072, 4096, AUX_NT); FENCE();
  u32x4 dw[5];
  {
    const rsrc_t rD = mkr(wd, 4608u * 1152u), rO = mkr(wo, 4096u * 1152u);
    const u32 co = (u32)w * 128u + (l & 7) * 16;
    const u32 od = w < 9 ? (u32)(18 * u + (l >> 3)) * 1152u + co : 0xFFFF0000u;
    const u32 oo = w < 9 ? (u32)(16 * u + (l >> 3)) * 1152u + co : 0xFFFF0000u;
    PV(4);
    THROTTLE();
    dw[0] = __builtin_amdgcn_raw_buffer_load_b128(rD, od, 0, AUX_NT);
    dw[1] = __builtin_amdgcn_raw_buffer_load_b128(rD, od, 8 * 1152, AUX_NT);
    dw[2] = __builtin_amdgcn_raw_buffer_load_b128(rD, l < 16 ? od : 0xFFFF0000u, 16 * 1152, AUX_NT);
    dw[3] = __builtin_amdgcn_raw_buffer_load_b128(rO, oo, 0, AUX_NT);
    dw[4] = __builtin_amdgcn_raw_buffer_load_b128(rO, oo, 8 * 1152, AUX_NT);
    FENCE();
  }
  if (w == 14) {
    u32x4 t[4];
    const u32 hb = WS_HX + (u32)u * 1152u + (l < 18 ? l : 0) * 16;
    for (u32 it = 0;; ++it) {
      asm volatile("" ::: "memory");
      bool ok = true;
#pragma unroll
      for (int i = 0; i < 4; ++i) t[i] = __builtin_amdgcn_raw_buffer_load_b128(rWS, hb + i * 288, 0, AUX_SC1);
#pragma unroll
      for (int i = 0; i < 4; ++i) ok &= tagok(t[i], tag);
      if (__all(ok) || it >= SPIN_G) break;
    }
    const float gs = ((__uint_as_float(t[0][0]) + __uint_as_float(t[1][0])) + __uint_as_float(t[2][0])) + __uint_as_float(t[3][0]);
    const float us = ((__uint_as_float(t[0][2]) + __uint_as_float(t[1][2])) + __uint_as_float(t[2][2])) + __uint_as_float(t[3][2]);
    const float hval = l < 18 ? gs / (1.0f + __expf(-gs)) * us : 0.f;
    u32 hb32 = __float_as_uint(hval); hb32 = hb32 == SENT ? 0x7FC00000u : hb32;
    if (l < 24) *(volatile u32*)&sm.hv[l] = hb32;
    STAMP(10);
    FENCE();
  }
  float fsc;
  {
    for (u32 it = 0; lds_ld(&sm.cm) != 16u && it < SPIN_L; ++it) __builtin_amdgcn_s_sleep(1);
    asm volatile("" ::: "memory");
    const f32x4* mp = (const f32x4*)&sm.wmax[l >> 5][0];
    const f32x4 m0 = mp[0], m1 = mp[1], m2 = mp[2], m3 = mp[3];
    float M = fmaxf(fmaxf(fmaxf(m0[0], m0[1]), fmaxf(m0[2], m0[3])), fmaxf(fmaxf(m1[0], m1[1]), fmaxf(m1[2], m1[3])));
    M = fmaxf(M, fmaxf(fmaxf(fmaxf(m2[0], m2[1]), fmaxf(m2[2], m2[3])), fmaxf(fmaxf(m3[0], m3[1]), fmaxf(m3[2], m3[3]))));
    fsc = __builtin_amdgcn_exp2f((l < 32 ? m[0] : m[1]) - M);
    m[0] = M;
    float lt = red32(ls[0], ls[1]); lt = red16(lt, lt);
    if ((l & 31) == 0) sm.lsum[l >> 5][w] = lt * fsc;
    FENCE();
  }
  PV(5); PV(6); PV(7);
  {
    float a8[8];
#pragma unroll
    for (int e = 0; e < 8; ++e) a8[e] = red32(acc[0][e], acc[1][e]);
    f32x4 a4;
#pragma unroll
    for (int e = 0; e < 4; ++e) a4[e] = red16(a8[e], a8[e + 4]) * fsc;
    *(f32x4*)&sm.acc[w][(l >> 5) * 128 + 8 * (l & 15) + 4 * ((l >> 4) & 1)] = a4;
    LDSWAIT();
    u32 old = 0;
    if (l == 0) old = __hip_atomic_fetch_add(&sm.ca, 1u, __ATOMIC_RELAXED, __HIP_MEMORY_SCOPE_WORKGROUP);
    old = __builtin_amdgcn_readfirstlane(old);
    if (w == 0) STAMP(11);
    if (old == 15u) {
      asm volatile("" ::: "memory");
      f32x4 t = *(const f32x4*)&sm.acc[0][4 * l];
#pragma unroll
      for (int i = 1; i < 16; ++i) t += *(const f32x4*)&sm.acc[i][4 * l];
      const f32x4* lp = (const f32x4*)&sm.lsum[l >> 5][0];
      const f32x4 l4 = (lp[0] + lp[1]) + (lp[2] + lp[3]);
      const float L = (l4[0] + l4[1]) + (l4[2] + l4[3]);
      const u32 ob = WS_OX + (u32)((g * 16 + (l >> 2)) * 16 + j) * 144u;
      __builtin_amdgcn_raw_buffer_store_b128(gran(t[0], t[1], tag), rWS, ob + (l & 3) * 32, 0, AUX_SC1);
      __builtin_amdgcn_raw_buffer_store_b128(gran(t[2], t[3], tag), rWS, ob + (l & 3) * 32 + 16, 0, AUX_SC1);
      if ((l & 3) == 0) __builtin_amdgcn_raw_buffer_store_b128(gran(m[0], L, tag), rWS, ob + 128, 0, AUX_SC1);
      STAMP(12);
    }
    FENCE();
  }

  if (w < 9) {
    const int rs = l >> 3;
    u32 h0, h1, h2;
    for (u32 it = 0;; ++it) {
      h0 = lds_ld(&sm.hv[rs]); h1 = lds_ld(&sm.hv[8 + rs]); h2 = lds_ld(&sm.hv[16 + rs]);
      if (__all(h0 != SENT && h1 != SENT && h2 != SENT) || it >= SPIN_L) break;
      __builtin_amdgcn_s_sleep(1);
    }
    float y[8];
#pragma unroll
    for (int e = 0; e < 4; ++e) {
      y[2 * e] = __uint_as_float(h0) * bflo(dw[0][e]) + __uint_as_float(h1) * bflo(dw[1][e]) + __uint_as_float(h2) * bflo(dw[2][e]);
      y[2 * e + 1] = __uint_as_float(h0) * bfhi(dw[0][e]) + __uint_as_float(h1) * bfhi(dw[1][e]) + __uint_as_float(h2) * bfhi(dw[2][e]);
    }
    if (w == 0) STAMP(13);
    u32 o0, o1;
    for (u32 it = 0;; ++it) {
      o0 = lds_ld(&sm.ov[rs]); o1 = lds_ld(&sm.ov[8 + rs]);
      if (__all(o0 != SENT && o1 != SENT) || it >= SPIN_L) break;
      __builtin_amdgcn_s_sleep(1);
    }
#pragma unroll
    for (int e = 0; e < 4; ++e) {
      y[2 * e] += __uint_as_float(o0) * bflo(dw[3][e]) + __uint_as_float(o1) * bflo(dw[4][e]);
      y[2 * e + 1] += __uint_as_float(o0) * bfhi(dw[3][e]) + __uint_as_float(o1) * bfhi(dw[4][e]);
    }
    float a4[4], a2[2];
#pragma unroll
    for (int e = 0; e < 4; ++e) a4[e] = red32(y[e], y[e + 4]);
#pragma unroll
    for (int e = 0; e < 2; ++e) { a2[e] = red16(a4[e], a4[e + 2]); a2[e] += dpp<0x128>(a2[e]); }
    const u32 cidx = 16 * w + 2 * (l & 7) + (l >> 5);
    if ((l & 8) == 0) __builtin_amdgcn_raw_buffer_store_b128(gran(a2[0], a2[1], tag), rWS, WS_FX + (cidx * 256u + (u32)u) * 32u + ((l >> 4) & 1) * 16, 0, AUX_SC1);
    if (w == 0) STAMP(14);
  } else if (w >= 12) {
    const u32 ob = WS_OX + (u32)(u * 16 + (l >> 2)) * 144u;
    u32x4 d0, d1, ml;
    bool done = false;
    for (u32 it = 0; it < SPIN_G; ++it) {
      asm volatile("" ::: "memory");
      d0 = __builtin_amdgcn_raw_buffer_load_b128(rWS, ob + (l & 3) * 32, 0, AUX_SC1);
      d1 = __builtin_amdgcn_raw_buffer_load_b128(rWS, ob + (l & 3) * 32 + 16, 0, AUX_SC1);
      ml = __builtin_amdgcn_raw_buffer_load_b128(rWS, ob + 128, 0, AUX_SC1);
      if (__all(tagok(d0, tag) && tagok(d1, tag) && tagok(ml, tag))) break;
      if (lds_ld(&sm.ov[0]) != SENT) { done = true; break; }
    }
    if (!done) {
      const float mp = __uint_as_float(ml[0]);
      float M = fmaxf(mp, dpp<0x124>(mp)); M = fmaxf(M, dpp<0x128>(M)); M = max32(max16(M));
      const float wgt = __builtin_amdgcn_exp2f(mp - M);
      float r[5] = {wgt * __uint_as_float(d0[0]), wgt * __uint_as_float(d0[2]), wgt * __uint_as_float(d1[0]), wgt * __uint_as_float(d1[2]),
                    wgt * __uint_as_float(ml[2])};
#pragma unroll
      for (int e = 0; e < 5; ++e) { r[e] += dpp<0x124>(r[e]); r[e] += dpp<0x128>(r[e]); r[e] = red16(r[e], r[e]); r[e] = red32(r[e], r[e]); }
      const float inv = 1.0f / r[4];
      u32x4 ovv;
#pragma unroll
      for (int e = 0; e < 4; ++e) { u32 t = __float_as_uint(r[e] * inv); ovv[e] = t == SENT ? 0x7FC00000u : t; }
      if (l < 4) *(volatile u32x4*)&sm.ov[4 * l] = ovv;
      STAMP(15 + (w - 12));
    }
  } else if (j < 9) {
    for (u32 it = 0; lds_ld(&sm.ov[0]) == SENT && it < SPIN_L; ++it) __builtin_amdgcn_s_sleep(2);
    const u32 cidx = (u32)(g * 9 + j);
    const u32 fb = WS_FX + cidx * 8192u + l * 16;
    u32x4 t[8];
    for (u32 it = 0; it < SPIN_G; ++it) {
      asm volatile("" ::: "memory");
      bool ok = true;
#pragma unroll
      for (int i = 0; i < 8; ++i) t[i] = __builtin_amdgcn_raw_buffer_load_b128(rWS, fb + (i & 3) * 1024, (i >> 2) * 4096, AUX_SC1);
#pragma unroll
      for (int i = 0; i < 8; ++i) ok &= tagok(t[i], tag);
      if (__all(ok)) break;
    }
    float a = 0.f, c = 0.f;
#pragma unroll
    for (int i = 0; i < 8; ++i) { a += __uint_as_float(t[i][0]); c += __uint_as_float(t[i][2]); }
    a += dpp<0x4E>(a); c += dpp<0x4E>(c);
    a += dpp<0x124>(a); c += dpp<0x124>(c);
    a += dpp<0x128>(a); c += dpp<0x128>(c);
    a = red16(a, a); c = red16(c, c);
    a = red32(a, a); c = red32(c, c);
    if (l < 2) {
      const u32 xr = __builtin_amdgcn_raw_buffer_load_b32(rX, cidx * 8 + l * 4, 0, 0);
      const bf16x2 r = {(__bf16)(bflo(xr) + a), (__bf16)(bfhi(xr) + c)};
      *(u32*)(out + cidx * 4 + l * 2) = __builtin_bit_cast(u32, r);
      if (cidx == 0 && l == 0) __builtin_amdgcn_raw_buffer_store_b32(Ev + 1u, rWS, WS_E, 0, AUX_SC1);
    }
    STAMP(19 + (w - 9));
  }
}

extern "C" void kernel_launch(void* const* d_in, const int* in_sizes, int n_in, void* d_out, int out_size, void* d_ws, size_t ws_size,
                              hipStream_t stream) {
  fused_decode<<<256, 1024, 0, stream>>>((const unsigned short*)d_in[0], (const unsigned short*)d_in[1], (const unsigned short*)d_in[2],
                                         (const unsigned short*)d_in[3], (const unsigned short*)d_in[4], (const unsigned short*)d_in[6],
                                         (const unsigned short*)d_in[7], (const unsigned short*)d_in[8], (unsigned char*)d_ws,
                                         (unsigned short*)d_out);
}
```

```cpp
#include <hip/hip_runtime.h>
#include <hip/hip_bf16.h>
#include <cstdint>

typedef unsigned u32;
typedef u32 u32x4 __attribute__((ext_vector_type(4)));
typedef u32 u32x2 __attribute__((ext_vector_type(2)));
typedef u32 u32x16 __attribute__((ext_vector_type(16)));
typedef float f32x4 __attribute__((ext_vector_type(4)));
typedef float f32x2 __attribute__((ext_vector_type(2)));
typedef __bf16 bf16x2 __attribute__((ext_vector_type(2)));
typedef __amdgpu_buffer_rsrc_t rsrc_t;

#define WIN 3

constexpr u32 WS_E = 0;
constexpr u32 WS_QX = 4096;
constexpr u32 WS_HX = WS_QX + 256 * 512;
constexpr u32 WS_ML = WS_HX + 256 * 1152;
constexpr u32 WS_OX = WS_ML + 16 * 512;
constexpr u32 WS_FX = WS_OX + 256 * 1024;
constexpr u32 WS_END = WS_FX + 72 * 8192;
constexpr u32 SPIN_G = 40000;
constexpr u32 SPIN_L = 4000000;
constexpr u32 SENT = 0xFFFFFFFFu;

#define STAMP(i) do { } while (0)

#define AUX_NT 2
#define AUX_SC1 16
#define FENCE() __builtin_amdgcn_sched_barrier(0)
#define THROTTLE() do { asm volatile("s_waitcnt vmcnt(%0)" :: "n"(WIN - 1) : "memory"); FENCE(); } while (0)
#define LDSWAIT() asm volatile("s_waitcnt lgkmcnt(0)" ::: "memory")

__device__ __forceinline__ float bflo(u32 u) { return __uint_as_float(u << 16); }
__device__ __forceinline__ float bfhi(u32 u) { return __uint_as_float(u & 0xffff0000u); }
__device__ __forceinline__ float dot2(u32 a, u32 b, float c) {
  return __builtin_amdgcn_fdot2_f32_bf16(__builtin_bit_cast(bf16x2, a), __builtin_bit_cast(bf16x2, b), c, false);
}
template <int CTRL> __device__ __forceinline__ float dpp(float x) {
  return __int_as_float(__builtin_amdgcn_update_dpp(0, __float_as_int(x), CTRL, 0xf, 0xf, true));
}
__device__ __forceinline__ float red32(float a, float b) {
  auto r = __builtin_amdgcn_permlane32_swap(__float_as_uint(a), __float_as_uint(b), false, false);
  return __uint_as_float(r[0]) + __uint_as_float(r[1]);
}
__device__ __forceinline__ float red16(float a, float b) {
  auto r = __builtin_amdgcn_permlane16_swap(__float_as_uint(a), __float_as_uint(b), false, false);
  return __uint_as_float(r[0]) + __uint_as_float(r[1]);
}
__device__ __forceinline__ float max32(float a) {
  auto r = __builtin_amdgcn_permlane32_swap(__float_as_uint(a), __float_as_uint(a), false, false);
  return fmaxf(__uint_as_float(r[0]), __uint_as_float(r[1]));
}
__device__ __forceinline__ float max16(float a) {
  auto r = __builtin_amdgcn_permlane16_swap(__float_as_uint(a), __float_as_uint(a), false, false);
  return fmaxf(__uint_as_float(r[0]), __uint_as_float(r[1]));
}
__device__ __forceinline__ float rowsum16(float s) {
  s += dpp<0xB1>(s); s += dpp<0x4E>(s); s += dpp<0x141>(s); s += dpp<0x140>(s);
  return s;
}
__device__ __forceinline__ float rowmax16(float s) {
  s = fmaxf(s, dpp<0xB1>(s)); s = fmaxf(s, dpp<0x4E>(s)); s = fmaxf(s, dpp<0x141>(s)); s = fmaxf(s, dpp<0x140>(s));
  return s;
}
__device__ __forceinline__ float wavesum(float s) { s = rowsum16(s); s = red16(s, s); s = red32(s, s); return s; }
__device__ __forceinline__ rsrc_t mkr(const void* p, u32 n) { return __builtin_amdgcn_make_buffer_rsrc((void*)p, 0, n, 0x00020000); }
__device__ __forceinline__ u32x4 gran(float a, float b, u32 tag) { u32x4 r = {__float_as_uint(a), tag, __float_as_uint(b), tag}; return r; }
__device__ __forceinline__ bool tagok(u32x4 v, u32 tag) { return v[1] == tag && v[3] == tag; }
typedef __attribute__((address_space(3))) volatile u32 lds_u32;
typedef __attribute__((address_space(3))) volatile u32x4 lds_u32x4;
__device__ __forceinline__ u32 lds_ld(u32* p) { return *(lds_u32*)p; }
__device__ __forceinline__ u32x4 lds_ld4(u32* p) { return *(lds_u32x4*)p; }
__device__ __forceinline__ void lds_st(u32* p, u32 v) { *(lds_u32*)p = v; }
#define PIN8(a) asm volatile("" : "+v"(a[0]), "+v"(a[1]), "+v"(a[2]), "+v"(a[3]), "+v"(a[4]), "+v"(a[5]), "+v"(a[6]), "+v"(a[7]))
__device__ __forceinline__ u32 nosent(float v) { const u32 t = __float_as_uint(v); return t == SENT ? 0x7FC00000u : t; }
__device__ __forceinline__ u32 arrive(u32* c, int l) {
  u32 old = 0;
  LDSWAIT();
  if (l == 0) old = __hip_atomic_fetch_add(c, 1u, __ATOMIC_RELAXED, __HIP_MEMORY_SCOPE_WORKGROUP);
  old = __builtin_amdgcn_readfirstlane(old);
  asm volatile("" ::: "memory");
  return old;
}

struct __align__(16) Smem {
  float qp[16][64];
  float hp[48][144];
  float acc[16][256];
  float wmax[2][16];
  float wl[2][16];
  u32 qv[2][2][64];
  u32 op[256];
  u32 fin[64];
  u32 hv[24];
  u32 wgt[16];
  u32 cq, ch, cm, ca, fq, pad0, pad1, pad2;
};

__global__ void __launch_bounds__(1024, 4)
fused_decode(const unsigned short* __restrict__ x, const unsigned short* __restrict__ Kc, const unsigned short* __restrict__ Vc,
             const unsigned short* __restrict__ wq, const unsigned short* __restrict__ wo, const unsigned short* __restrict__ wg,
             const unsigned short* __restrict__ wu, const unsigned short* __restrict__ wd, unsigned char* __restrict__ ws,
             unsigned short* __restrict__ out) {
  __shared__ Smem sm;
  const int b = blockIdx.x;
  const int w = __builtin_amdgcn_readfirstlane(threadIdx.x >> 6);
  const int l = threadIdx.x & 63;
  const int u = (b & 7) * 32 + (b >> 3);
  const int g = u >> 4, j = u & 15, k4 = u & 3;
  const bool fcons = (u & 31) < 9;
  const u32 cidx = (u32)((u >> 5) * 9 + (u & 31));
  const rsrc_t rX = mkr(x, 1152), rK = mkr(Kc, 33554432u), rV = mkr(Vc, 33554432u), rWS = mkr(ws, WS_END);
  const int r0 = 144 * k4;

  if (w == 15) {
    const u32x4 sv = {SENT, SENT, SENT, SENT};
    __builtin_amdgcn_raw_buffer_store_b128(sv, rWS, WS_OX + (u32)u * 1024u + l * 16, 0, AUX_SC1);
    if (fcons) {
#pragma unroll
      for (int i = 0; i < 8; ++i) __builtin_amdgcn_raw_buffer_store_b128(sv, rWS, WS_FX + cidx * 8192u + l * 16 + (i & 3) * 1024, (i >> 2) * 4096, AUX_SC1);
    }
  }
  const u32x4 xa = __builtin_amdgcn_raw_buffer_load_b128(rX, l * 16, 0, 0);
  const u32 xb = __builtin_amdgcn_raw_buffer_load_b32(rX, 1024 + l * 4, 0, 0);
  const u32 Ev = __builtin_amdgcn_raw_buffer_load_b32(rWS, WS_E, 0, AUX_SC1);
  const int rq = r0 + 9 * w + (l >> 3);
  const u32 xq0 = __builtin_amdgcn_raw_buffer_load_b16(rX, rq * 2, 0, 0);
  const rsrc_t rQ = mkr(wq, 576u * 8192u);
  const u32 qcol = (u32)(256 * g + 64 * (j >> 2)) * 2 + (l & 7) * 16;
  const u32 kvoff = (u32)u * 131072u + (u32)w * 8192u + (u32)l * 16u;

  if (threadIdx.x < 368) {
    u32* z = sm.op;
    z[threadIdx.x] = threadIdx.x < 360 ? SENT : 0u;
  }

  u32x4 kk[8], vv[8];
  float mean, rstd;
  u32 tag;
#define LN_STATS() do {                                                                                   \
    float s_ = 0.f;                                                                                       \
    _Pragma("unroll") for (int e = 0; e < 4; ++e) s_ += bflo(xa[e]) + bfhi(xa[e]);                        \
    s_ += bflo(xb) + bfhi(xb);                                                                            \
    mean = wavesum(s_) * (1.0f / 576.0f);                                                                 \
    float v_ = 0.f;                                                                                       \
    _Pragma("unroll") for (int e = 0; e < 4; ++e) { float d0 = bflo(xa[e]) - mean, d1 = bfhi(xa[e]) - mean; v_ += d0 * d0 + d1 * d1; } \
    { float d0 = bflo(xb) - mean, d1 = bfhi(xb) - mean; v_ += l < 32 ? d0 * d0 + d1 * d1 : 0.f; }         \
    rstd = 1.0f / sqrtf(wavesum(v_) * (1.0f / 576.0f) + 1e-5f);                                           \
    tag = __builtin_amdgcn_readfirstlane(Ev) * 0x9E3779B1u + 0x7F4A7C15u;                                 \
  } while (0)
#define QPART(P) do {                                                                                     \
    float a4[4], a2[2];                                                                                   \
    _Pragma("unroll") for (int e = 0; e < 4; ++e) a4[e] = red32(P[e], P[e + 4]);                          \
    _Pragma("unroll") for (int e = 0; e < 2; ++e) a2[e] = red16(a4[e], a4[e + 2]);                        \
    float t_ = (l & 8) ? a2[0] : a2[1], kp_ = (l & 8) ? a2[1] : a2[0];                                    \
    float a1 = kp_ + dpp<0x128>(t_);                                                                      \
    sm.qp[w][8 * (l & 7) + 4 * (l >> 5) + 2 * ((l >> 4) & 1) + ((l >> 3) & 1)] = a1;                      \
    if (arrive(&sm.cq, l) == 15u) {                                                                       \
      const int P_ = l & 31, hf = l >> 5;                                                                 \
      f32x2 s_ = {0.f, 0.f};                                                                              \
      _Pragma("unroll") for (int i = 0; i < 8; ++i) s_ += *(const f32x2*)&sm.qp[hf * 8 + i][2 * P_];      \
      const float s0 = red32(s_[0], s_[0]), s1 = red32(s_[1], s_[1]);                                     \
      if (l < 32) __builtin_amdgcn_raw_buffer_store_b128(gran(s0, s1, tag), rWS, WS_QX + (u32)u * 512u + P_ * 16, 0, AUX_SC1); \
      STAMP(3);                                                                                           \
    }                                                                                                     \
  } while (0)

  typedef const __attribute__((address_space(1))) u32x4* gptr_t;
  const int gi = l & 31, gslot = gi / 9, c9 = gi - gslot * 9;
  const int rg = r0 + 9 * w + (gi < 27 ? gslot : 0);
  unsigned long long gpa = (unsigned long long)(l < 32 ? wg : wu) + (u32)rg * 9216u + (u32)(u >> 2) * 144u + c9 * 16;
  asm volatile("" : "+v"(gpa));
  u32 xg[3];
#pragma unroll
  for (int i = 0; i < 3; ++i) xg[i] = __builtin_amdgcn_raw_buffer_load_b16(rX, (rg + 3 * i) * 2, 0, 0);
  const u32 xq1 = __builtin_amdgcn_raw_buffer_load_b16(rX, (r0 + 9 * w + 8) * 2, 0, 0);
  FENCE();
  const u32x4 q0 = __builtin_amdgcn_raw_buffer_load_b128(rQ, (u32)rq * 8192u + qcol, 0, AUX_NT); FENCE();
  const u32x4 q1 = __builtin_amdgcn_raw_buffer_load_b128(rQ, l < 8 ? (u32)rq * 8192u + qcol : 0xFFF00000u, 8 * 8192, AUX_NT); FENCE();
  u32x4 gu[3];
  gu[0] = __builtin_nontemporal_load((gptr_t)gpa); FENCE();
  gu[1] = __builtin_nontemporal_load((gptr_t)(gpa + 3 * 9216)); FENCE();
  LDSWAIT(); __builtin_amdgcn_s_barrier(); FENCE();
  LN_STATS();
  THROTTLE();
  gu[2] = __builtin_nontemporal_load((gptr_t)(gpa + 6 * 9216)); FENCE();
  {
    const float xn = (bflo(xq0) - mean) * rstd, xn1 = (bflo(xq1) - mean) * rstd;
    float p[8];
#pragma unroll
    for (int e = 0; e < 4; ++e) { p[2 * e] = xn * bflo(q0[e]) + xn1 * bflo(q1[e]); p[2 * e + 1] = xn * bfhi(q0[e]) + xn1 * bfhi(q1[e]); }
    QPART(p);
  }
  if (w == 0) STAMP(1);
  {
    float ga[8];
#pragma unroll
    for (int e = 0; e < 8; ++e) ga[e] = 0.f;
#pragma unroll
    for (int i = 0; i < 3; ++i) {
      THROTTLE(); kk[i] = __builtin_amdgcn_raw_buffer_load_b128(rK, kvoff + i * 1024, 0, AUX_NT); FENCE();
      const float xn = (bflo(xg[i]) - mean) * rstd;
#pragma unroll
      for (int e = 0; e < 4; ++e) { ga[2 * e] += xn * bflo(gu[i][e]); ga[2 * e + 1] += xn * bfhi(gu[i][e]); }
      PIN8(ga);
    }
    if (gi < 27) {
      float* hp = &sm.hp[w * 3 + gslot][(l >> 5) * 72 + 8 * c9];
      *(f32x4*)hp = f32x4{ga[0], ga[1], ga[2], ga[3]};
      *(f32x4*)(hp + 4) = f32x4{ga[4], ga[5], ga[6], ga[7]};
    }
    LDSWAIT();
    if (l == 0) __hip_atomic_fetch_add(&sm.ch, 1u, __ATOMIC_RELAXED, __HIP_MEMORY_SCOPE_WORKGROUP);
    if (w == 0) STAMP(2);
    FENCE();
  }

#define THR13() do { if (w != 13) asm volatile("s_waitcnt vmcnt(%0)" :: "n"(WIN - 1) : "memory"); FENCE(); } while (0)
  THR13(); kk[3] = __builtin_amdgcn_raw_buffer_load_b128(rK, kvoff + 3072, 0, AUX_NT); FENCE();
  THR13(); kk[4] = __builtin_amdgcn_raw_buffer_load_b128(rK, kvoff, 4096, AUX_NT); FENCE();
  THR13(); kk[5] = __builtin_amdgcn_raw_buffer_load_b128(rK, kvoff + 1024, 4096, AUX_NT); FENCE();
  THR13(); kk[6] = __builtin_amdgcn_raw_buffer_load_b128(rK, kvoff + 2048, 4096, AUX_NT); FENCE();
  THR13(); kk[7] = __builtin_amdgcn_raw_buffer_load_b128(rK, kvoff + 3072, 4096, AUX_NT); FENCE();
  if (w == 13) {
    STAMP(4);
    const unsigned char* qf = ws + WS_QX + (u32)g * 8192u;
    for (u32 it = 0; it < SPIN_G; ++it) {
      u32x2 f[16];
#pragma unroll
      for (int i = 0; i < 16; ++i) asm volatile("s_load_dwordx2 %0, %1, %2 glc" : "=&s"(f[i]) : "s"(qf), "n"(i * 512 + 504) : "memory");
      asm volatile("s_waitcnt lgkmcnt(0)" ::: "memory");
      u32 bad = 0;
#pragma unroll
      for (int i = 0; i < 16; ++i) { asm volatile("" : "+s"(f[i])); bad |= f[i][1] ^ tag; }
      if (bad == 0) break;
    }
    STAMP(16);
    u32x4 t[8];
    const u32 qb = WS_QX + (u32)g * 8192u + (u32)(l >> 5) * 2048u + (l & 31) * 16;
    for (u32 it = 0;; ++it) {
      asm volatile("" ::: "memory");
      bool ok = true;
#pragma unroll
      for (int i = 0; i < 8; ++i) t[i] = __builtin_amdgcn_raw_buffer_load_b128(rWS, qb + (i & 3) * 512, (i >> 2) * 4096, AUX_SC1);
#pragma unroll
      for (int i = 0; i < 8; ++i) ok &= tagok(t[i], tag);
      if (__all(ok) || it >= SPIN_G) break;
    }
    const float qs = 0.08838834764831845f * 1.4426950408889634f;
#pragma unroll
    for (int h = 0; h < 2; ++h) {
      float a = ((__uint_as_float(t[4 * h][0]) + __uint_as_float(t[4 * h + 1][0])) + __uint_as_float(t[4 * h + 2][0])) + __uint_as_float(t[4 * h + 3][0]);
      float c = ((__uint_as_float(t[4 * h][2]) + __uint_as_float(t[4 * h + 1][2])) + __uint_as_float(t[4 * h + 2][2])) + __uint_as_float(t[4 * h + 3][2]);
      a *= qs; c *= qs;
      const bf16x2 hi = {(__bf16)a, (__bf16)c};
      const u32 hib = __builtin_bit_cast(u32, hi);
      const bf16x2 lo = {(__bf16)(a - bflo(hib)), (__bf16)(c - bfhi(hib))};
      sm.qv[h][0][l] = hib;
      sm.qv[h][1][l] = __builtin_bit_cast(u32, lo);
    }
    LDSWAIT();
    if (l == 0) lds_st(&sm.fq, 1u);
    STAMP(5);
    FENCE();
  }
  THROTTLE(); vv[0] = __builtin_amdgcn_raw_buffer_load_b128(rV, kvoff, 0, AUX_NT); FENCE();
  if (w >= 12) {
    for (u32 it = 0; lds_ld(&sm.ch) != 16u && it < SPIN_L; ++it) __builtin_amdgcn_s_sleep(1);
    asm volatile("" ::: "memory");
    const int v = gi < 18 ? gi : 0, hf = l >> 5, cw = w - 12;
    const int col = (v >= 9 ? 72 + 2 * (v - 9) : 2 * v) + 18 * cw;
    f32x2 sa = {0.f, 0.f}, sb = {0.f, 0.f};
#pragma unroll
    for (int i = 0; i < 24; i += 2) { sa += *(const f32x2*)&sm.hp[hf * 24 + i][col]; sb += *(const f32x2*)&sm.hp[hf * 24 + i + 1][col]; }
    sa += sb;
    const float s0 = red32(sa[0], sa[0]), s1 = red32(sa[1], sa[1]);
    if (l < 18) __builtin_amdgcn_raw_buffer_store_b128(gran(s0, s1, tag), rWS, WS_HX + (u32)((u >> 2) * 4 + cw) * 1152u + k4 * 288 + l * 16, 0, AUX_SC1);
    if (w == 13) STAMP(6);
    FENCE();
  }
  THROTTLE(); vv[1] = __builtin_amdgcn_raw_buffer_load_b128(rV, kvoff + 1024, 0, AUX_NT); FENCE();
  if (w == 0) STAMP(7);
  for (u32 it = 0; lds_ld(&sm.fq) == 0u && it < SPIN_L; ++it) __builtin_amdgcn_s_sleep(1);
  asm volatile("" ::: "memory");
  if (w == 0) STAMP(8);
  float s[8][2];
  {
    const int c = l & 15;
    const u32x4 qh0 = *(const u32x4*)&sm.qv[0][0][4 * c], ql0 = *(const u32x4*)&sm.qv[0][1][4 * c];
    const u32x4 qh1 = *(const u32x4*)&sm.qv[1][0][4 * c], ql1 = *(const u32x4*)&sm.qv[1][1][4 * c];
#pragma unroll
    for (int i = 0; i < 8; ++i) {
      if (i == 3) { THROTTLE(); vv[2] = __builtin_amdgcn_raw_buffer_load_b128(rV, kvoff + 2048, 0, AUX_NT); FENCE(); }
      if (i == 6) { THROTTLE(); vv[3] = __builtin_amdgcn_raw_buffer_load_b128(rV, kvoff + 3072, 0, AUX_NT); FENCE(); }
      float a0 = 0.f, a1 = 0.f;
#pragma unroll
      for (int e = 0; e < 4; ++e) { a0 = dot2(kk[i][e], ql0[e], a0); a1 = dot2(kk[i][e], ql1[e], a1); }
#pragma unroll
      for (int e = 0; e < 4; ++e) { a0 = dot2(kk[i][e], qh0[e], a0); a1 = dot2(kk[i][e], qh1[e], a1); }
      s[i][0] = rowsum16(a0); s[i][1] = rowsum16(a1);
      asm volatile("" : "+v"(s[i][0]), "+v"(s[i][1]));
    }
  }
  float mw;
  {
    float m[2], ls[2];
#pragma unroll
    for (int h = 0; h < 2; ++h) {
      float mm = fmaxf(fmaxf(fmaxf(s[0][h], s[1][h]), fmaxf(s[2][h], s[3][h])), fmaxf(fmaxf(s[4][h], s[5][h]), fmaxf(s[6][h], s[7][h])));
      mm = max32(max16(mm));
      m[h] = mm;
      float t = 0.f;
#pragma unroll
      for (int i = 0; i < 8; ++i) { s[i][h] = __builtin_amdgcn_exp2f(s[i][h] - mm); t += s[i][h]; }
      ls[h] = t;
    }
    mw = l < 32 ? m[0] : m[1];
    float lt = red32(ls[0], ls[1]); lt = red16(lt, lt);
    if ((l & 31) == 0) { sm.wmax[l >> 5][w] = mw; sm.wl[l >> 5][w] = lt; }
    if (arrive(&sm.cm, l) == 15u) {
      const int h = (l >> 4) & 1;
      const float mi = sm.wmax[h][l & 15], li = sm.wl[h][l & 15];
      const float M = rowmax16(mi);
      const float L = rowsum16(li * __builtin_amdgcn_exp2f(mi - M));
      if ((l & 47) == 0) __builtin_amdgcn_raw_buffer_store_b128(gran(M, L, tag), rWS, WS_ML + (u32)(g * 2 + h) * 256u + j * 16, 0, AUX_SC1);
      STAMP(17);
    }
  }
  if (w == 0) STAMP(9);
  FENCE();
  float acc[2][8];
#pragma unroll
  for (int e = 0; e < 8; ++e) { acc[0][e] = 0.f; acc[1][e] = 0.f; }
#define PV(i) do {                                                                                                    \
    _Pragma("unroll") for (int e = 0; e < 4; ++e) {                                                                   \
      const float v0 = bflo(vv[i][e]), v1 = bfhi(vv[i][e]);                                                           \
      acc[0][2 * e] += s[i][0] * v0; acc[0][2 * e + 1] += s[i][0] * v1;                                               \
      acc[1][2 * e] += s[i][1] * v0; acc[1][2 * e + 1] += s[i][1] * v1; }                                             \
    PIN8(acc[0]); PIN8(acc[1]); FENCE(); } while (0)
  THROTTLE(); vv[4] = __builtin_amdgcn_raw_buffer_load_b128(rV, kvoff, 4096, AUX_NT); FENCE();
  PV(0); PV(1);
  THROTTLE(); vv[5] = __builtin_amdgcn_raw_buffer_load_b128(rV, kvoff + 1024, 4096, AUX_NT); FENCE();
  PV(2);
  THROTTLE(); vv[6] = __builtin_amdgcn_raw_buffer_load_b128(rV, kvoff + 2048, 4096, AUX_NT); FENCE();
  PV(3);
  THROTTLE(); vv[7] = __builtin_amdgcn_raw_buffer_load_b128(rV, kvoff + 3072, 4096, AUX_NT); FENCE();
  PV(4);
  float fsc;
  {
    for (u32 it = 0; lds_ld(&sm.cm) != 16u && it < SPIN_L; ++it) __builtin_amdgcn_s_sleep(1);
    asm volatile("" ::: "memory");
    const float M = rowmax16(sm.wmax[l >> 5][l & 15]);
    fsc = __builtin_amdgcn_exp2f(mw - M);
    FENCE();
  }
  u32x4 dw[5];
  const rsrc_t rD = mkr(wd, 4608u * 1152u), rO = mkr(wo, 4096u * 1152u);
  const u32 co = (u32)w * 128u + (l & 7) * 16;
  const u32 od = w < 9 ? (u32)(18 * u + (l >> 3)) * 1152u + co : 0xFFFF0000u;
  const u32 oo = w < 9 ? (u32)(16 * u + (l >> 3)) * 1152u + co : 0xFFFF0000u;
  THROTTLE(); dw[0] = __builtin_amdgcn_raw_buffer_load_b128(rD, od, 0, AUX_NT); FENCE();
  PV(5);
  THROTTLE(); dw[1] = __builtin_amdgcn_raw_buffer_load_b128(rD, od, 8 * 1152, AUX_NT); FENCE();
  PV(6);
  THROTTLE();
  dw[2] = __builtin_amdgcn_raw_buffer_load_b128(rD, l < 16 ? od : 0xFFFF0000u, 16 * 1152, AUX_NT);
  dw[3] = __builtin_amdgcn_raw_buffer_load_b128(rO, oo, 0, AUX_NT); FENCE();
  PV(7);
  {
    float a8[8];
#pragma unroll
    for (int e = 0; e < 8; ++e) a8[e] = red32(acc[0][e], acc[1][e]);
    f32x4 a4;
#pragma unroll
    for (int e = 0; e < 4; ++e) a4[e] = red16(a8[e], a8[e + 4]) * fsc;
    *(f32x4*)&sm.acc[w][(l >> 5) * 128 + 8 * (l & 15) + 4 * ((l >> 4) & 1)] = a4;
    const u32 old = arrive(&sm.ca, l);
    if (w == 0) STAMP(11);
    if (old == 15u) {
      f32x4 t0 = *(const f32x4*)&sm.acc[0][4 * l], t1 = *(const f32x4*)&sm.acc[1][4 * l];
#pragma unroll
      for (int i = 2; i < 16; i += 2) { t0 += *(const f32x4*)&sm.acc[i][4 * l]; t1 += *(const f32x4*)&sm.acc[i + 1][4 * l]; }
      t0 += t1;
      const u32x4 r = {nosent(t0[0]), nosent(t0[1]), nosent(t0[2]), nosent(t0[3])};
      __builtin_amdgcn_raw_buffer_store_b128(r, rWS, WS_OX + (u32)(g * 16 + (l >> 2)) * 1024u + j * 64 + (l & 3) * 16, 0, AUX_SC1);
      STAMP(12);
    }
    FENCE();
  }
  dw[4] = __builtin_amdgcn_raw_buffer_load_b128(rO, oo, 8 * 1152, AUX_NT); FENCE();

  if (w == 14) {
    u32x4 t[2];
    const int v = (l & 31) < 18 ? (l & 31) : 0;
    const u32 hb = WS_HX + (u32)u * 1152u + (l >> 5) * 288 + v * 16;
    for (u32 it = 0;; ++it) {
      asm volatile("" ::: "memory");
      t[0] = __builtin_amdgcn_raw_buffer_load_b128(rWS, hb, 0, AUX_SC1);
      t[1] = __builtin_amdgcn_raw_buffer_load_b128(rWS, hb + 576, 0, AUX_SC1);
      if (__all(tagok(t[0], tag) && tagok(t[1], tag)) || it >= SPIN_G) break;
    }
    float c0 = __uint_as_float(t[0][0]) + __uint_as_float(t[1][0]), c1 = __uint_as_float(t[0][2]) + __uint_as_float(t[1][2]);
    c0 = red32(c0, c0); c1 = red32(c1, c1);
    const int src = (l < 18 ? l >> 1 : 0) * 4;
    const int g0 = __builtin_amdgcn_ds_bpermute(src, __float_as_int(c0)), g1 = __builtin_amdgcn_ds_bpermute(src, __float_as_int(c1));
    const int u0 = __builtin_amdgcn_ds_bpermute(src + 36, __float_as_int(c0)), u1 = __builtin_amdgcn_ds_bpermute(src + 36, __float_as_int(c1));
    const float gs = __int_as_float((l & 1) ? g1 : g0), us = __int_as_float((l & 1) ? u1 : u0);
    const float hval = l < 18 ? gs / (1.0f + __expf(-gs)) * us : 0.f;
    if (l < 24) lds_st(&sm.hv[l], nosent(hval));
    STAMP(10);
  } else if (w == 12) {
    u32x4 t;
    for (u32 it = 0;; ++it) {
      asm volatile("" ::: "memory");
      t = __builtin_amdgcn_raw_buffer_load_b128(rWS, WS_ML + (u32)(g * 2 + (j >> 3)) * 256u + (l & 15) * 16, 0, AUX_SC1);
      if (__all(tagok(t, tag)) || it >= SPIN_G) break;
    }
    const float mi = __uint_as_float(t[0]);
    const float e = __builtin_amdgcn_exp2f(mi - rowmax16(mi));
    const float L = rowsum16(e * __uint_as_float(t[2]));
    if (l < 16) lds_st(&sm.wgt[l], nosent(e / L));
    STAMP(18);
  } else if (w == 15) {
    const int c0 = l, c1 = 64 + (l & 7);
    const u32 a0 = WS_QX + (u32)((c0 / 9) * 32 + c0 % 9) * 512u, a1 = WS_QX + (u32)((c1 / 9) * 32 + c1 % 9) * 512u;
    for (u32 it = 0;; ++it) {
      asm volatile("" ::: "memory");
      const u32x4 t0 = __builtin_amdgcn_raw_buffer_load_b128(rWS, a0, 0, AUX_SC1);
      const u32x4 t1 = __builtin_amdgcn_raw_buffer_load_b128(rWS, a1, 0, AUX_SC1);
      if (__all(tagok(t0, tag) && tagok(t1, tag)) || it >= SPIN_G) break;
    }
    STAMP(19);
  }
  FENCE();
  float y[8];
  if (w < 9) {
    const int rs = l >> 3;
    u32 h0, h1, h2;
    for (u32 it = 0;; ++it) {
      h0 = lds_ld(&sm.hv[rs]); h1 = lds_ld(&sm.hv[8 + rs]); h2 = lds_ld(&sm.hv[16 + rs]);
      if (__all(h0 != SENT && h1 != SENT && h2 != SENT) || it >= SPIN_L) break;
      __builtin_amdgcn_s_sleep(1);
    }
#pragma unroll
    for (int e = 0; e < 4; ++e) {
      y[2 * e] = __uint_as_float(h0) * bflo(dw[0][e]) + __uint_as_float(h1) * bflo(dw[1][e]) + __uint_as_float(h2) * bflo(dw[2][e]);
      y[2 * e + 1] = __uint_as_float(h0) * bfhi(dw[0][e]) + __uint_as_float(h1) * bfhi(dw[1][e]) + __uint_as_float(h2) * bfhi(dw[2][e]);
    }
    if (w == 0) STAMP(13);
  }
  FENCE();
  if (w >= 9) {
    const int p0 = w - 9;
    u32 wb;
    for (u32 it = 0;; ++it) {
      wb = lds_ld(&sm.wgt[(p0 + 7 * (l >> 4)) & 15]);
      if (__all(wb != SENT) || it >= SPIN_L) break;
      __builtin_amdgcn_s_sleep(1);
    }
    const unsigned char* op = ws + WS_OX + (u32)u * 1024u + (u32)p0 * 64u;
    u32 pend = p0 < 2 ? 7u : 3u;
    for (u32 it = 0; it < SPIN_G && pend; ++it) {
      u32x16 v[3];
      asm volatile("s_load_dwordx16 %0, %1, 0x0 glc" : "=&s"(v[0]) : "s"(op) : "memory");
      asm volatile("s_load_dwordx16 %0, %1, 0x1c0 glc" : "=&s"(v[1]) : "s"(op) : "memory");
      if (p0 < 2) asm volatile("s_load_dwordx16 %0, %1, 0x380 glc" : "=&s"(v[2]) : "s"(op) : "memory");
      asm volatile("s_waitcnt lgkmcnt(0)" ::: "memory");
#pragma unroll
      for (int k = 0; k < 3; ++k) {
        if (pend & (1u << k)) {
          asm volatile("" : "+s"(v[k]));
          u32 mx = v[k][0];
#pragma unroll
          for (int e = 1; e < 16; ++e) mx = mx > v[k][e] ? mx : v[k][e];
          if (mx != SENT) {
            pend &= ~(1u << k);
            u32 t = 0;
#pragma unroll
            for (int e = 0; e < 16; ++e) asm("v_writelane_b32 %0, %1, %2" : "+v"(t) : "s"(v[k][e]), "n"(16 * k + e));
            const u32 r = nosent(__uint_as_float(t) * __uint_as_float(wb));
            const int p = p0 + 7 * k;
            if ((l >> 4) == k) lds_st(&sm.op[((l & 7) * 8 + (p >> 1)) * 4 + ((l >> 3) & 1) * 2 + (p & 1)], r);
          }
        }
      }
    }
    if (pend) { if (l < 64) { lds_st(&sm.op[4 * l], 0u); lds_st(&sm.op[4 * l + 1], 0u); lds_st(&sm.op[4 * l + 2], 0u); lds_st(&sm.op[4 * l + 3], 0u); } }
    if (w == 9) STAMP(20);
    if (w == 13) STAMP(15);
  }
  if (w < 9) {
    float wf[16];
#pragma unroll
    for (int e = 0; e < 4; ++e) { wf[2 * e] = bflo(dw[3][e]); wf[2 * e + 1] = bfhi(dw[3][e]); wf[8 + 2 * e] = bflo(dw[4][e]); wf[9 + 2 * e] = bfhi(dw[4][e]); }
    if (w == 0) STAMP(21);
    u32x4 o4;
    for (u32 it = 0;; ++it) {
      o4 = lds_ld4(&sm.op[4 * l]);
      if (__all(o4[0] != SENT && o4[1] != SENT && o4[2] != SENT && o4[3] != SENT) || it >= SPIN_L) break;
    }
    float o0 = __uint_as_float(o4[0]) + __uint_as_float(o4[1]), o1 = __uint_as_float(o4[2]) + __uint_as_float(o4[3]);
    o0 += dpp<0xB1>(o0); o1 += dpp<0xB1>(o1);
    o0 += dpp<0x4E>(o0); o1 += dpp<0x4E>(o1);
    o0 += dpp<0x141>(o0); o1 += dpp<0x141>(o1);
#pragma unroll
    for (int e = 0; e < 8; ++e) y[e] += o0 * wf[e] + o1 * wf[8 + e];
    float a4[4], a2[2];
#pragma unroll
    for (int e = 0; e < 4; ++e) a4[e] = red32(y[e], y[e + 4]);
#pragma unroll
    for (int e = 0; e < 2; ++e) a2[e] = red16(a4[e], a4[e + 2]);
    const float t_ = (l & 8) ? a2[0] : a2[1], kp_ = (l & 8) ? a2[1] : a2[0];
    const float a1 = kp_ + dpp<0x128>(t_);
    __builtin_amdgcn_raw_buffer_store_b32(nosent(a1), rWS, WS_FX + (u32)(8 * w + (l & 7)) * 8192u + (u32)u * 32u +
                                          (4 * (l >> 5) + 2 * ((l >> 4) & 1) + ((l >> 3) & 1)) * 4, 0, AUX_SC1);
    if (w == 0) STAMP(14);
  }
  if (fcons) {
    const int pw = w & 7;
    if (w >= 9) {
      for (u32 it = 0; it < SPIN_L; ++it) {
        const u32x4 o4 = lds_ld4(&sm.op[4 * l]);
        if (__all(o4[0] != SENT && o4[1] != SENT && o4[2] != SENT && o4[3] != SENT)) break;
        __builtin_amdgcn_s_sleep(2);
      }
    }
#define FDELAY 4
    __builtin_amdgcn_s_sleep(FDELAY);
    if (w >= 8) __builtin_amdgcn_s_sleep(5);
    const u32 xr = __builtin_amdgcn_raw_buffer_load_b16(rX, cidx * 16 + (l & 7) * 2, 0, 0);
    const u32 fb = WS_FX + cidx * 8192u + pw * 1024 + l * 16;
    u32x4 t;
    bool have = false;
    for (u32 it = 0; it < SPIN_G; ++it) {
      asm volatile("" ::: "memory");
      t = __builtin_amdgcn_raw_buffer_load_b128(rWS, fb, 0, AUX_SC1);
      if (__all(t[0] != SENT && t[1] != SENT && t[2] != SENT && t[3] != SENT)) { have = true; break; }
      if (lds_ld(&sm.fin[pw * 8]) != SENT) break;
    }
    if (have) {
      const float b0 = red32(__uint_as_float(t[0]), __uint_as_float(t[2])), b1 = red32(__uint_as_float(t[1]), __uint_as_float(t[3]));
      float c = red16(b0, b1);
      c += dpp<0x4E>(c); c += dpp<0x124>(c); c += dpp<0x128>(c);
      if ((l & 14) == 0) lds_st(&sm.fin[pw * 8 + 4 * (l & 1) + 2 * (l >> 5) + ((l >> 4) & 1)], nosent(c));
    }
    if (w == 0) STAMP(22);
    u32 f;
    for (u32 it = 0;; ++it) {
      f = lds_ld(&sm.fin[l]);
      if (__all(f != SENT) || it >= SPIN_L) break;
    }
    float c = __uint_as_float(f);
    c += dpp<0x128>(c); c = red16(c, c); c = red32(c, c);
    const __bf16 r = (__bf16)(bflo(xr) + c);
    if (l < 8) out[cidx * 8 + l] = __builtin_bit_cast(unsigned short, r);
    if (cidx == 0 && l == 0) __builtin_amdgcn_raw_buffer_store_b32(Ev + 1u, rWS, WS_E, 0, AUX_SC1);
    if (w == 0) STAMP(23);
  }
}

extern "C" void kernel_launch(void* const* d_in, const int* in_sizes, int n_in, void* d_out, int out_size, void* d_ws, size_t ws_size,
                              hipStream_t stream) {
  fused_decode<<<256, 1024, 0, stream>>>((const unsigned short*)d_in[0], (const unsigned short*)d_in[1], (const unsigned short*)d_in[2],
                                         (const unsigned short*)d_in[3], (const unsigned short*)d_in[4], (const unsigned short*)d_in[6],
                                         (const unsigned short*)d_in[7], (const unsigned short*)d_in[8], (unsigned char*)d_ws,
                                         (unsigned short*)d_out);
}
```

```cpp
#include <hip/hip_runtime.h>
#include <hip/hip_bf16.h>
#include <cstdint>

typedef unsigned u32;
typedef u32 u32x4 __attribute__((ext_vector_type(4)));
typedef u32 u32x2 __attribute__((ext_vector_type(2)));
typedef u32 u32x16 __attribute__((ext_vector_type(16)));
typedef float f32x4 __attribute__((ext_vector_type(4)));
typedef float f32x2 __attribute__((ext_vector_type(2)));
typedef __bf16 bf16x2 __attribute__((ext_vector_type(2)));
typedef __bf16 bf16x8 __attribute__((ext_vector_type(8)));
typedef __amdgpu_buffer_rsrc_t rsrc_t;

#define WIN 3

constexpr u32 WS_E = 0;
constexpr u32 WS_QX = 4096;
constexpr u32 WS_HX = WS_QX + 256 * 512;
constexpr u32 WS_FL = WS_HX + 256 * 1152;
constexpr u32 WS_AG = WS_FL + 256 * 128;
constexpr u32 WS_ML = WS_AG + 16 * 512;
constexpr u32 WS_OX = WS_ML + 256 * 128;
constexpr u32 WS_FX = WS_OX + 256 * 1024;
constexpr u32 WS_END = WS_FX + 72 * 8192;
constexpr u32 SPIN_G = 40000;
constexpr u32 SPIN_L = 4000000;
constexpr u32 SENT = 0xFFFFFFFFu;
constexpr u32 OOR = 0xFFF00000u;

#define STAMP(i) do { } while (0)
#define WST(id) do { } while (0)

#define AUX_NT 2
#define AUX_SC1 16
#define FENCE() __builtin_amdgcn_sched_barrier(0)
#define THR(n) do { asm volatile("s_waitcnt vmcnt(%0)" :: "n"(n) : "memory"); FENCE(); } while (0)
#define THROTTLE() THR(WIN - 1)
#define LDSWAIT() asm volatile("s_waitcnt lgkmcnt(0)" ::: "memory")

__device__ __forceinline__ float bflo(u32 u) { return __uint_as_float(u << 16); }
__device__ __forceinline__ float bfhi(u32 u) { return __uint_as_float(u & 0xffff0000u); }
template <int CTRL> __device__ __forceinline__ float dpp(float x) {
  return __int_as_float(__builtin_amdgcn_update_dpp(0, __float_as_int(x), CTRL, 0xf, 0xf, true));
}
template <int N> __device__ __forceinline__ float rorf(float x) {
  if constexpr ((N & 15) == 0) return x; else return dpp<0x120 + (N & 15)>(x);
}
template <int N> __device__ __forceinline__ u32 roru(u32 x) {
  if constexpr ((N & 15) == 0) return x; else return (u32)__builtin_amdgcn_update_dpp(0, (int)x, 0x120 + (N & 15), 0xf, 0xf, true);
}
__device__ __forceinline__ float red32(float a, float b) {
  auto r = __builtin_amdgcn_permlane32_swap(__float_as_uint(a), __float_as_uint(b), false, false);
  return __uint_as_float(r[0]) + __uint_as_float(r[1]);
}
__device__ __forceinline__ float red16(float a, float b) {
  auto r = __builtin_amdgcn_permlane16_swap(__float_as_uint(a), __float_as_uint(b), false, false);
  return __uint_as_float(r[0]) + __uint_as_float(r[1]);
}
__device__ __forceinline__ float max32(float a) {
  auto r = __builtin_amdgcn_permlane32_swap(__float_as_uint(a), __float_as_uint(a), false, false);
  return fmaxf(__uint_as_float(r[0]), __uint_as_float(r[1]));
}
__device__ __forceinline__ float max16(float a) {
  auto r = __builtin_amdgcn_permlane16_swap(__float_as_uint(a), __float_as_uint(a), false, false);
  return fmaxf(__uint_as_float(r[0]), __uint_as_float(r[1]));
}
__device__ __forceinline__ float rowsum16(float s) {
  s += dpp<0xB1>(s); s += dpp<0x4E>(s); s += dpp<0x141>(s); s += dpp<0x140>(s);
  return s;
}
__device__ __forceinline__ float rowmax16(float s) {
  s = fmaxf(s, dpp<0xB1>(s)); s = fmaxf(s, dpp<0x4E>(s)); s = fmaxf(s, dpp<0x141>(s)); s = fmaxf(s, dpp<0x140>(s));
  return s;
}
__device__ __forceinline__ float wavesum(float s) { s = rowsum16(s); s = red16(s, s); s = red32(s, s); return s; }
__device__ __forceinline__ rsrc_t mkr(const void* p, u32 n) { return __builtin_amdgcn_make_buffer_rsrc((void*)p, 0, n, 0x00020000); }
__device__ __forceinline__ u32x4 gran(float a, float b, u32 tag) { u32x4 r = {__float_as_uint(a), tag, __float_as_uint(b), tag}; return r; }
__device__ __forceinline__ bool tagok(u32x4 v, u32 tag) { return v[1] == tag && v[3] == tag; }
typedef __attribute__((address_space(3))) volatile u32 lds_u32;
typedef __attribute__((address_space(3))) volatile u32x4 lds_u32x4;
typedef __attribute__((address_space(3))) volatile unsigned short lds_u16;
__device__ __forceinline__ u32 lds_ld(u32* p) { return *(lds_u32*)p; }
__device__ __forceinline__ u32x4 lds_ld4(u32* p) { return *(lds_u32x4*)p; }
__device__ __forceinline__ void lds_st(u32* p, u32 v) { *(lds_u32*)p = v; }
#define PIN8(a) asm volatile("" : "+v"(a[0]), "+v"(a[1]), "+v"(a[2]), "+v"(a[3]), "+v"(a[4]), "+v"(a[5]), "+v"(a[6]), "+v"(a[7]))
__device__ __forceinline__ u32 nosent(float v) { const u32 t = __float_as_uint(v); return t == SENT ? 0x7FC00000u : t; }
__device__ __forceinline__ u32 arrive(u32* c, int l) {
  u32 old = 0;
  LDSWAIT();
  if (l == 0) old = __hip_atomic_fetch_add(c, 1u, __ATOMIC_RELAXED, __HIP_MEMORY_SCOPE_WORKGROUP);
  old = __builtin_amdgcn_readfirstlane(old);
  asm volatile("" ::: "memory");
  return old;
}
__device__ __forceinline__ f32x4 mfma(u32x4 a, u32x4 b, f32x4 c) {
  return __builtin_amdgcn_mfma_f32_16x16x32_bf16(__builtin_bit_cast(bf16x8, a), __builtin_bit_cast(bf16x8, b), c, 0, 0, 0);
}

struct __align__(16) Smem {
  float qp[8][64];
  float hp[36][144];
  float acc[8][256];
  float wmax[2][8];
  float wl[2][8];
  u32 qv[2][2][64];
  u32 xs[320];
  float xn[640];
  u32 op[256];
  u32 fin[64];
  u32 hv[24];
  u32 wgt[16];
  u32 cq, ch, cm, ca, fq, fx, cv, ff;
  u32 ev, pad3, pad4, pad5;
};
#define WA 3
#define NA 6
#define WB 6
#define NB 32
#define WC 6
#define NC 32
#define WD 6
#define BARI 2
#define TRIGN 8u
#define QSLP 4
#define HSLP 8
#define AUX_1ST 0
#define MSLP 4
#define AUX_E 0
#define WS_ 3
#define QPOS 7
#define TRIG 15
#define BUMP(c) do { if (l == 0) __hip_atomic_fetch_add(&sm.c, 1u, __ATOMIC_RELAXED, __HIP_MEMORY_SCOPE_WORKGROUP); } while (0)
#define LWAIT(c, n) do { for (u32 it = 0; lds_ld(&sm.c) != (n) && it < SPIN_L; ++it) __builtin_amdgcn_s_sleep(1); asm volatile("" ::: "memory"); } while (0)

__global__ void __launch_bounds__(1024, 4)
fused_decode(const unsigned short* __restrict__ x, const unsigned short* __restrict__ Kc, const unsigned short* __restrict__ Vc,
             const unsigned short* __restrict__ wq, const unsigned short* __restrict__ wo, const unsigned short* __restrict__ wg,
             const unsigned short* __restrict__ wu, const unsigned short* __restrict__ wd, unsigned char* __restrict__ ws,
             unsigned short* __restrict__ out) {
  __shared__ Smem sm;
  const int b = blockIdx.x;
  const int w = __builtin_amdgcn_readfirstlane(threadIdx.x >> 6);
  const int l = threadIdx.x & 63;
#define PXA 0
#define PXB 0
  const int u = ((b + PXA) & 7) * 32 + ((b >> 3) ^ PXB);
  const int g = u >> 4, j = u & 15, k4 = u & 3;
  const bool fcons = (u & 31) < 9;
  const u32 cidx = (u32)((u >> 5) * 9 + (u & 31));
  const rsrc_t rWS = mkr(ws, WS_END);
  const bool ow = w < 9;
  const rsrc_t rD = mkr(wd, 4608u * 1152u), rO = mkr(wo, 4096u * 1152u);
  const u32 co = (u32)w * 128u + (l & 7) * 16;
  const u32 od = ow ? (u32)(18 * u + (l >> 3)) * 1152u + co : OOR;
  const u32 oo = ow ? (u32)(16 * u + (l >> 3)) * 1152u + co : OOR;
  u32x4 dw[5];
#define DWLOADS() do {                                                                                     \
    dw[0] = __builtin_amdgcn_raw_buffer_load_b128(rD, od, 0, AUX_NT);                                      \
    dw[1] = __builtin_amdgcn_raw_buffer_load_b128(rD, od, 8 * 1152, AUX_NT);                               \
    dw[2] = __builtin_amdgcn_raw_buffer_load_b128(rD, l < 16 ? od : OOR, 16 * 1152, AUX_NT);               \
    dw[3] = __builtin_amdgcn_raw_buffer_load_b128(rO, oo, 0, AUX_NT);                                      \
    dw[4] = __builtin_amdgcn_raw_buffer_load_b128(rO, oo, 8 * 1152, AUX_NT); FENCE(); } while (0)

  if (w < 8) {
    const rsrc_t rX = mkr(x, 1152), rQ = mkr(wq, 576u * 8192u), rG = mkr(wg, 576u * 9216u), rU = mkr(wu, 576u * 9216u);
    const int s6 = l / 9, c9 = l - 9 * s6;
    const int rb = 144 * k4 + 18 * w, rg = 144 * k4 + 24 * (w < 6 ? w : 0);
    const u32 qo = (u32)(rb + (l >> 3)) * 8192u + (u32)(256 * g + 64 * (j >> 2)) * 2 + (l & 7) * 16;
    const u32 go = l < 54 && w < 6 ? (u32)(rg + s6) * 9216u + (u32)(u >> 2) * 144u + c9 * 16 : OOR;
    u32x4 xa = {0u, 0u, 0u, 0u};
    u32 xb = 0u, Ev = 0u;
    if (w == 0) {
      xa = __builtin_amdgcn_raw_buffer_load_b128(rX, l * 16, 0, 0);
      xb = __builtin_amdgcn_raw_buffer_load_b32(rX, 1024 + l * 4, 0, 0);
      Ev = __builtin_amdgcn_raw_buffer_load_b32(rWS, WS_E, 0, AUX_E);
    }
    {
      const u32x4 sv = {SENT, SENT, SENT, SENT};
      if (w == 7) __builtin_amdgcn_raw_buffer_store_b128(sv, rWS, WS_OX + (u32)u * 1024u + l * 16, 0, AUX_SC1);
      if (w == 6) __builtin_amdgcn_raw_buffer_store_b128(sv, rWS, l < 8 ? WS_ML + (u32)u * 128u + l * 16 : OOR, 0, AUX_SC1);
      if (fcons) __builtin_amdgcn_raw_buffer_store_b128(sv, rWS, WS_FX + cidx * 8192u + (u32)w * 1024u + l * 16, 0, AUX_SC1);
    }
    FENCE();
    u32x4 fq_[3], fg[4], fu[4];
    fq_[0] = __builtin_amdgcn_raw_buffer_load_b128(rQ, qo, 0, AUX_NT); FENCE();
    fq_[1] = __builtin_amdgcn_raw_buffer_load_b128(rQ, qo, 8 * 8192, AUX_NT); FENCE();
    fq_[2] = __builtin_amdgcn_raw_buffer_load_b128(rQ, l < 16 ? qo : OOR, 16 * 8192, AUX_NT); FENCE();
    if (threadIdx.x < 368) sm.op[threadIdx.x] = threadIdx.x < 360 ? SENT : 0u;
    LDSWAIT(); __builtin_amdgcn_s_barrier(); FENCE(); WST(0);
    fg[0] = __builtin_amdgcn_raw_buffer_load_b128(rG, go, 0, AUX_NT); FENCE();
    if (w == 0) {
      float xf[10];
#pragma unroll
      for (int e = 0; e < 4; ++e) { xf[2 * e] = bflo(xa[e]); xf[2 * e + 1] = bfhi(xa[e]); }
      xf[8] = bflo(xb); xf[9] = bfhi(xb);
      const float mean = wavesum((((xf[0] + xf[1]) + (xf[2] + xf[3])) + ((xf[4] + xf[5]) + (xf[6] + xf[7]))) + (xf[8] + xf[9])) * (1.0f / 576.0f);
#pragma unroll
      for (int e = 0; e < 10; ++e) xf[e] -= mean;
      float v0 = xf[0] * xf[0], v1 = xf[1] * xf[1];
#pragma unroll
      for (int e = 2; e < 8; e += 2) { v0 += xf[e] * xf[e]; v1 += xf[e + 1] * xf[e + 1]; }
      v0 += l < 32 ? xf[8] * xf[8] + xf[9] * xf[9] : 0.f;
      const float rstd = __builtin_amdgcn_rsqf(wavesum(v0 + v1) * (1.0f / 576.0f) + 1e-5f);
      *(f32x4*)&sm.xn[8 * l] = f32x4{xf[0] * rstd, xf[1] * rstd, xf[2] * rstd, xf[3] * rstd};
      *(f32x4*)&sm.xn[8 * l + 4] = f32x4{xf[4] * rstd, xf[5] * rstd, xf[6] * rstd, xf[7] * rstd};
      *(f32x2*)&sm.xn[512 + 2 * l] = f32x2{xf[8] * rstd, xf[9] * rstd};
      *(u32x4*)&sm.xs[4 * l] = xa;
      sm.xs[256 + l] = xb;
      if (l == 0) sm.ev = Ev;
      LDSWAIT();
      if (l == 0) lds_st(&sm.fx, 1u);
      STAMP(2);
    }
    FENCE();
    LWAIT(fx, 1u);
    const u32 tag = __builtin_amdgcn_readfirstlane(sm.ev) * 0x9E3779B1u + 0x7F4A7C15u;
    float xq[3], xg[4];
#pragma unroll
    for (int i = 0; i < 3; ++i) xq[i] = sm.xn[rb + (l >> 3) + 8 * i];
#pragma unroll
    for (int i = 0; i < 4; ++i) xg[i] = sm.xn[rg + (l < 54 ? s6 : 0) + 6 * i];
    asm volatile("" : "+v"(xq[0]), "+v"(xq[1]), "+v"(xq[2]), "+v"(xg[0]), "+v"(xg[1]), "+v"(xg[2]), "+v"(xg[3]));
    FENCE();
    THR(3); fg[1] = __builtin_amdgcn_raw_buffer_load_b128(rG, go, 6 * 9216, AUX_NT); FENCE();
    THR(2); fg[2] = __builtin_amdgcn_raw_buffer_load_b128(rG, go, 12 * 9216, AUX_NT); FENCE();
    WST(1);
#define FACC(N, G, F, X) do {                                                                                          \
      _Pragma("unroll") for (int e = 0; e < 4; ++e) G[e] = f32x2{X[0], X[0]} * f32x2{bflo(F[0][e]), bfhi(F[0][e])};    \
      _Pragma("unroll") for (int i = 1; i < N; ++i)                                                                    \
        _Pragma("unroll") for (int e = 0; e < 4; ++e) G[e] += f32x2{X[i], X[i]} * f32x2{bflo(F[i][e]), bfhi(F[i][e])}; \
    } while (0)
    {
      f32x2 g2[4];
      FACC(3, g2, fq_, xq);
      float a4[4], a2[2];
#pragma unroll
      for (int e = 0; e < 4; ++e) a4[e] = red32(g2[e >> 1][e & 1], g2[2 + (e >> 1)][e & 1]);
#pragma unroll
      for (int e = 0; e < 2; ++e) a2[e] = red16(a4[e], a4[e + 2]);
      const float t_ = (l & 8) ? a2[0] : a2[1], kp_ = (l & 8) ? a2[1] : a2[0];
      sm.qp[w][8 * (l & 7) + 4 * (l >> 5) + 2 * ((l >> 4) & 1) + ((l >> 3) & 1)] = kp_ + dpp<0x128>(t_);
      if (w == 0) STAMP(1);
      if (arrive(&sm.cq, l) == 7u) {
        const int P_ = l & 31, hf = l >> 5;
        const f32x2 s_ = (*(const f32x2*)&sm.qp[hf * 4][2 * P_] + *(const f32x2*)&sm.qp[hf * 4 + 1][2 * P_]) +
                         (*(const f32x2*)&sm.qp[hf * 4 + 2][2 * P_] + *(const f32x2*)&sm.qp[hf * 4 + 3][2 * P_]);
        const float s0 = red32(s_[0], s_[0]), s1 = red32(s_[1], s_[1]);
        if (l < 32) __builtin_amdgcn_raw_buffer_store_b128(gran(s0, s1, tag), rWS, WS_QX + (u32)u * 512u + P_ * 16, 0, AUX_SC1);
        const u32 fa = l < 16 ? WS_FL + (u32)(g * 16 + l) * 128u + j * 4 : (fcons && l < 32) ? WS_AG + (u32)(l - 16) * 512u + cidx * 4u : OOR;
        __builtin_amdgcn_raw_buffer_store_b32(tag, rWS, fa, 0, AUX_SC1);
        STAMP(3);
      }
      FENCE();
    }
    if (w < 6) {
      float* const hp = &sm.hp[w * 6 + (l < 54 ? s6 : 0)][8 * c9];
      THR(WS_ - 1); fg[3] = __builtin_amdgcn_raw_buffer_load_b128(rG, go, 18 * 9216, AUX_NT); FENCE();
      THR(WS_ - 1); fu[0] = __builtin_amdgcn_raw_buffer_load_b128(rU, go, 0, AUX_NT); FENCE();
      THR(WS_ - 1); fu[1] = __builtin_amdgcn_raw_buffer_load_b128(rU, go, 6 * 9216, AUX_NT); FENCE();
      THR(WS_ - 1); fu[2] = __builtin_amdgcn_raw_buffer_load_b128(rU, go, 12 * 9216, AUX_NT); FENCE();
      THR(WS_ - 1); fu[3] = __builtin_amdgcn_raw_buffer_load_b128(rU, go, 18 * 9216, AUX_NT); FENCE();
      f32x2 g2[4], u2[4];
      FACC(4, g2, fg, xg);
      if (l < 54) {
        *(f32x4*)hp = f32x4{g2[0][0], g2[0][1], g2[1][0], g2[1][1]};
        *(f32x4*)(hp + 4) = f32x4{g2[2][0], g2[2][1], g2[3][0], g2[3][1]};
      }
      FENCE();
      FACC(4, u2, fu, xg);
      if (l < 54) {
        *(f32x4*)(hp + 72) = f32x4{u2[0][0], u2[0][1], u2[1][0], u2[1][1]};
        *(f32x4*)(hp + 76) = f32x4{u2[2][0], u2[2][1], u2[3][0], u2[3][1]};
      }
      LDSWAIT();
      BUMP(ch);
      WST(2);
      if (w < 4) {
        LWAIT(ch, 6u);
        const int gi = l & 31, v = gi < 18 ? gi : 0, hf = l >> 5;
        const int col = (v >= 9 ? 72 + 2 * (v - 9) : 2 * v) + 18 * w;
        f32x2 sa = {0.f, 0.f}, sb = {0.f, 0.f};
#pragma unroll
        for (int i = 0; i < 18; i += 2) { sa += *(const f32x2*)&sm.hp[hf * 18 + i][col]; sb += *(const f32x2*)&sm.hp[hf * 18 + i + 1][col]; }
        sa += sb;
        const float s0 = red32(sa[0], sa[0]), s1 = red32(sa[1], sa[1]);
        if (l < 18) __builtin_amdgcn_raw_buffer_store_b128(gran(s0, s1, tag), rWS, WS_HX + (u32)((u >> 2) * 4 + w) * 1152u + k4 * 288 + l * 16, 0, AUX_SC1);
        if (w == 0) STAMP(6);
      }
    } else {
      if (w == 6) {
        const unsigned char* fp = ws + WS_FL + (u32)u * 128u;
        LWAIT(cq, 8u);
        for (u32 it = 0; it < SPIN_G; ++it) {
          u32x16 v;
          __builtin_amdgcn_s_sleep(QSLP);
          asm volatile("s_load_dwordx16 %0, %1, 0x0 glc\n s_waitcnt lgkmcnt(0)" : "=&s"(v) : "s"(fp) : "memory");
          u32 bad = 0;
#pragma unroll
          for (int e = 0; e < 16; ++e) bad |= v[e] ^ tag;
          if (bad == 0) break;
        }
        if (l == 0) lds_st(&sm.ff, 1u);
      } else {
        LWAIT(ff, 1u);
      }
      if (w == 6) STAMP(4);
      const u32 g1a = WS_QX + (u32)(g * 16 + (w - 6) * 8 + (l >> 4)) * 512u + (l & 15) * 16;
      u32x4 g1[4];
#pragma unroll
      for (int i = 0; i < 4; ++i) g1[i] = __builtin_amdgcn_raw_buffer_load_b128(rWS, g1a + (i & 1) * 256 + (i >> 1) * 2048, 0, AUX_1ST);
      asm volatile("" : "+v"(g1[0]), "+v"(g1[1]), "+v"(g1[2]), "+v"(g1[3]));
      if (w == 6) STAMP(16);
      if (!__all(tagok(g1[0], tag) && tagok(g1[1], tag) && tagok(g1[2], tag) && tagok(g1[3], tag))) {
        for (u32 it = 0;; ++it) {
          asm volatile("" ::: "memory");
#pragma unroll
          for (int i = 0; i < 4; ++i) g1[i] = __builtin_amdgcn_raw_buffer_load_b128(rWS, g1a + (i & 1) * 256 + (i >> 1) * 2048, 0, AUX_SC1);
          asm volatile("" : "+v"(g1[0]), "+v"(g1[1]), "+v"(g1[2]), "+v"(g1[3]));
          if (__all(tagok(g1[0], tag) && tagok(g1[1], tag) && tagok(g1[2], tag) && tagok(g1[3], tag)) || it >= SPIN_G) break;
        }
      }
      const float qs = 0.08838834764831845f * 1.4426950408889634f;
#pragma unroll
      for (int i = 0; i < 4; ++i) {
        float a = red16(__uint_as_float(g1[i][0]), __uint_as_float(g1[i][2]));
        a = red32(a, a) * qs;
        const u32 hib = __builtin_bit_cast(unsigned short, (__bf16)a);
        const u32 lob = __builtin_bit_cast(unsigned short, (__bf16)(a - bflo(hib)));
        *((lds_u16*)&sm.qv[w - 6][l >> 5][16 * i + (l & 15)] + ((l >> 4) & 1)) = (unsigned short)(l < 32 ? hib : lob);
      }
      LDSWAIT();
      BUMP(fq);
      if (w == 6) STAMP(5);
      WST(3);
    }
    FENCE();
    if (w == 7) {
      const int v = (l & 31) < 18 ? (l & 31) : 0;
      const u32 hb = WS_HX + (u32)u * 1152u + (l >> 5) * 288 + v * 16;
      const unsigned char* hs = ws + WS_HX + (u32)u * 1152u;
      LWAIT(ch, 6u);
      for (u32 it = 0; it < SPIN_G; ++it) {
        u32 a0, a1, a2, a3;
        __builtin_amdgcn_s_sleep(HSLP);
        asm volatile("s_load_dword %0, %4, 0x11c glc\n s_load_dword %1, %4, 0x23c glc\n s_load_dword %2, %4, 0x35c glc\n s_load_dword %3, %4, 0x47c glc\n s_waitcnt lgkmcnt(0)"
                     : "=&s"(a0), "=&s"(a1), "=&s"(a2), "=&s"(a3) : "s"(hs) : "memory");
        if (((a0 ^ tag) | (a1 ^ tag) | (a2 ^ tag) | (a3 ^ tag)) == 0) break;
      }
      u32x4 t0, t1;
      for (u32 it = 0;; ++it) {
        asm volatile("" ::: "memory");
        const u32x4 e0 = __builtin_amdgcn_raw_buffer_load_b128(rWS, WS_AG + (u32)g * 512u + v * 16, 0, AUX_SC1);
        t0 = __builtin_amdgcn_raw_buffer_load_b128(rWS, hb, 0, AUX_SC1);
        t1 = __builtin_amdgcn_raw_buffer_load_b128(rWS, hb + 576, 0, AUX_SC1);
        if (__all(tagok(e0, tag) && e0[0] == tag && e0[2] == tag && tagok(t0, tag) && tagok(t1, tag)) || it >= SPIN_G) break;
        __builtin_amdgcn_s_sleep(8);
      }
      float c0 = __uint_as_float(t0[0]) + __uint_as_float(t1[0]), c1_ = __uint_as_float(t0[2]) + __uint_as_float(t1[2]);
      c0 = red32(c0, c0); c1_ = red32(c1_, c1_);
      const int src = (l < 18 ? l >> 1 : 0) * 4;
      const int g0 = __builtin_amdgcn_ds_bpermute(src, __float_as_int(c0)), g1 = __builtin_amdgcn_ds_bpermute(src, __float_as_int(c1_));
      const int u0 = __builtin_amdgcn_ds_bpermute(src + 36, __float_as_int(c0)), u1 = __builtin_amdgcn_ds_bpermute(src + 36, __float_as_int(c1_));
      const float gs = __int_as_float((l & 1) ? g1 : g0), us = __int_as_float((l & 1) ? u1 : u0);
      const float hval = l < 18 ? gs / (1.0f + __expf(-gs)) * us : 0.f;
      if (l < 24) lds_st(&sm.hv[l], nosent(hval));
      STAMP(10);
    } else if (w == 6) {
      LWAIT(cm, 8u);
      const int h = (l >> 3) & 1;
      const float mi = sm.wmax[h][l & 7], li = sm.wl[h][l & 7];
      float M = fmaxf(mi, dpp<0xB1>(mi)); M = fmaxf(M, dpp<0x4E>(M)); M = fmaxf(M, dpp<0x141>(M));
      float L = li * __builtin_amdgcn_exp2f(mi - M);
      L += dpp<0xB1>(L); L += dpp<0x4E>(L); L += dpp<0x141>(L);
      const u32x2 ml = {nosent(M), nosent(L)};
      __builtin_amdgcn_raw_buffer_store_b64(ml, rWS, l < 16 ? WS_ML + (u32)(g * 16 + l) * 128u + j * 8 : OOR, 0, AUX_SC1);
      STAMP(17);
    }
    FENCE();
    for (u32 it = 0; lds_ld(&sm.cv) < TRIGN && it < SPIN_L; ++it) __builtin_amdgcn_s_sleep(1);
    asm volatile("" ::: "memory");
    DWLOADS();
    if (w == 0) STAMP(7);
    if (w == 1) {
      const unsigned char* mp = ws + WS_ML + (u32)u * 128u;
      u32x16 v[2];
      for (u32 it = 0; it < SPIN_G; ++it) {
        asm volatile("s_load_dwordx16 %0, %1, 0x0 glc" : "=&s"(v[0]) : "s"(mp) : "memory");
        asm volatile("s_load_dwordx16 %0, %1, 0x40 glc" : "=&s"(v[1]) : "s"(mp) : "memory");
        asm volatile("s_waitcnt lgkmcnt(0)" ::: "memory");
        u32 mx = 0;
#pragma unroll
        for (int k = 0; k < 2; ++k) {
          asm volatile("" : "+s"(v[k]));
#pragma unroll
          for (int e = 0; e < 16; ++e) mx = mx > v[k][e] ? mx : v[k][e];
        }
        if (mx != SENT) break;
        __builtin_amdgcn_s_sleep(MSLP);
      }
      u32 tm = 0, tl = 0;
#pragma unroll
      for (int k = 0; k < 2; ++k)
#pragma unroll
        for (int e = 0; e < 8; ++e) {
          asm("v_writelane_b32 %0, %1, %2" : "+v"(tm) : "s"(v[k][2 * e]), "n"(8 * k + e));
          asm("v_writelane_b32 %0, %1, %2" : "+v"(tl) : "s"(v[k][2 * e + 1]), "n"(8 * k + e));
        }
      asm volatile("s_nop 1" : "+v"(tm), "+v"(tl));
      const float mi = __uint_as_float(tm);
      const float e = __builtin_amdgcn_exp2f(mi - rowmax16(mi));
      const float L = rowsum16(e * __uint_as_float(tl));
      if (l < 16) lds_st(&sm.wgt[l], nosent(e / L));
      STAMP(18);
    } else if (w == 2) {
      u32x4 t[8];
      for (u32 it = 0;; ++it) {
        const u32 c = lds_ld(&sm.ca);
#pragma unroll
        for (int i = 0; i < 8; ++i) t[i] = lds_ld4((u32*)&sm.acc[i][4 * l]);
        if (c == 8u || it >= SPIN_L) break;
      }
      f32x4 t0 = __builtin_bit_cast(f32x4, t[0]) + __builtin_bit_cast(f32x4, t[1]), t1 = __builtin_bit_cast(f32x4, t[2]) + __builtin_bit_cast(f32x4, t[3]);
      f32x4 t2 = __builtin_bit_cast(f32x4, t[4]) + __builtin_bit_cast(f32x4, t[5]), t3 = __builtin_bit_cast(f32x4, t[6]) + __builtin_bit_cast(f32x4, t[7]);
      t0 = (t0 + t1) + (t2 + t3);
      const u32x4 r = {nosent(t0[0]), nosent(t0[1]), nosent(t0[2]), nosent(t0[3])};
      __builtin_amdgcn_raw_buffer_store_b128(r, rWS, WS_OX + (u32)(g * 16 + (l >> 2)) * 1024u + j * 64 + (l & 3) * 16, 0, AUX_SC1);
      STAMP(12);
    }
    FENCE();
  } else {
    const rsrc_t rK = mkr(Kc, 33554432u), rV = mkr(Vc, 33554432u);
#define PKV 0
#define KVI 0
    const u32 kvb = (u32)(u ^ PKV) * 131072u + (u32)(w - 8) * 16384u;
#define TVO(i) (((i) & 3) * 1024)
#define TSO(i) (((i) >> 2) * 4096)
    const u32 voff = kvb + (u32)l * 16u;
    const u32 koff = kvb + (u32)(((l >> 2) & 3) * 256 + (l >> 4) * 64 + (l & 3) * 16);
    u32x4 kk[16], vv[16];
#define WINOF(n) ((n) < NA ? WA : (n) < NB ? WB : (n) < NC ? WC : WD)
#define KLD(i) do { if ((i) >= WA) THR(WINOF(i) - 1);                                                                      \
      kk[i] = __builtin_amdgcn_raw_buffer_load_b128(rK, koff + TVO(i), TSO(i), AUX_NT); FENCE();                            \
      if ((i) == BARI) { __builtin_amdgcn_s_barrier(); FENCE(); WST(0); } } while (0)
#define VLD(i) do { THR(WINOF(16 + (i)) - 1);                                                                               \
      vv[i] = __builtin_amdgcn_raw_buffer_load_b128(rV, voff + TVO(i), TSO(i), AUX_NT); FENCE();                            \
      if ((i) == TRIG) { BUMP(cv); FENCE(); } } while (0)
    KLD(0); KLD(1); KLD(2); KLD(3); KLD(4); KLD(5); KLD(6); KLD(7);
    WST(1);
    KLD(8); KLD(9); KLD(10); KLD(11); KLD(12); KLD(13); KLD(14); KLD(15);
    WST(2);
    VLD(0); VLD(1); VLD(2); VLD(3); VLD(4); VLD(5);
    if (QPOS >= 6) VLD(6);
    if (QPOS >= 7) VLD(7);
    WST(3);
    LWAIT(fq, 2u);
    if (w == 8) STAMP(8);
    WST(4);
    const int bank = (l >> 2) & 3;
    u32 pk[4];
    float mw;
    {
      const u32x4 qb = *(const u32x4*)&sm.qv[(l >> 1) & 1][l & 1][4 * ((l >> 4) * 4 + bank)];
      float sc[4];
#pragma unroll
      for (int ab = 0; ab < 4; ++ab) {
        const f32x4 z = {0.f, 0.f, 0.f, 0.f};
        const f32x4 d0 = mfma(kk[4 * ab], qb, z), d1 = mfma(kk[4 * ab + 1], qb, z), d2 = mfma(kk[4 * ab + 2], qb, z), d3 = mfma(kk[4 * ab + 3], qb, z);
        float a0 = ((d0[0] + rorf<-4>(d0[1])) + rorf<-8>(d0[2])) + rorf<-12>(d0[3]);
        float a1 = ((d1[1] + rorf<4>(d1[0])) + rorf<-4>(d1[2])) + rorf<-8>(d1[3]);
        float a2 = ((d2[2] + rorf<8>(d2[0])) + rorf<4>(d2[1])) + rorf<-4>(d2[3]);
        float a3 = ((d3[3] + rorf<12>(d3[0])) + rorf<8>(d3[1])) + rorf<4>(d3[2]);
        asm volatile("" : "+v"(a0), "+v"(a1), "+v"(a2), "+v"(a3));
        const float t01 = (l & 4) ? a1 : a0, t23 = (l & 4) ? a3 : a2;
        const float t = (l & 8) ? t23 : t01;
        sc[ab] = t + dpp<0xB1>(t);
        asm volatile("" : "+v"(sc[ab]));
        if (ab == 1) { FENCE(); VLD(QPOS + 1); }
      }
      FENCE(); VLD(QPOS + 2);
      float m = fmaxf(fmaxf(sc[0], sc[1]), fmaxf(sc[2], sc[3]));
      m = fmaxf(m, rorf<4>(m)); m = fmaxf(m, rorf<8>(m));
      m = max32(max16(m));
      mw = m;
#pragma unroll
      for (int ab = 0; ab < 4; ++ab) {
        const float p = __builtin_amdgcn_exp2f(sc[ab] - m);
        const bf16x2 t = {(__bf16)p, (__bf16)p};
        pk[ab] = __builtin_bit_cast(u32, t);
      }
      float lt = (bfhi(pk[0]) + bfhi(pk[1])) + (bfhi(pk[2]) + bfhi(pk[3]));
      lt += rorf<4>(lt); lt += rorf<8>(lt);
      lt = red16(lt, lt); lt = red32(lt, lt);
      if ((l & 61) == 0) { sm.wmax[l >> 1][w - 8] = mw; sm.wl[l >> 1][w - 8] = lt; }
      LDSWAIT();
      BUMP(cm);
    }
    if (w == 8) STAMP(9);
    WST(5);
    FENCE();
    const u32 hm = (l & 1) ? 0xffff0000u : 0x0000ffffu;
    const u32 m0 = bank == 0 ? hm : 0u, m1 = bank == 1 ? hm : 0u, m2 = bank == 2 ? hm : 0u, m3 = bank == 3 ? hm : 0u;
    u32x4 pb[16];
#define PBSET(t) do { pb[t][0] = roru<4 * (0 - ((t) & 3))>(pk[(t) >> 2]) & m0; pb[t][1] = roru<4 * (1 - ((t) & 3))>(pk[(t) >> 2]) & m1;   \
                      pb[t][2] = roru<4 * (2 - ((t) & 3))>(pk[(t) >> 2]) & m2; pb[t][3] = roru<4 * (3 - ((t) & 3))>(pk[(t) >> 2]) & m3;   \
                      asm volatile("" : "+v"(pb[t])); } while (0)
    f32x4 acc = {0.f, 0.f, 0.f, 0.f};
#define PV(i) do { acc = mfma(vv[i], pb[i], acc); asm volatile("" : "+v"(acc)); FENCE(); } while (0)
    if (QPOS < 6) VLD(8);
    if (QPOS < 7) VLD(9);
    PBSET(0); PBSET(1); PBSET(2); PBSET(3); PV(0); PV(1); PV(2); PV(3);
    VLD(10);
    PBSET(4); PBSET(5); PBSET(6); PBSET(7); PV(4);
    VLD(11);
    PV(5); PBSET(8); PBSET(9); PBSET(10); PBSET(11); FENCE();
    VLD(12);
    PV(6); PBSET(12); PBSET(13); PBSET(14); PBSET(15); FENCE();
    VLD(13);
    PV(7);
    VLD(14);
    PV(8);
    VLD(15);
    PV(9);
    DWLOADS();
    float fsc;
    {
      LWAIT(cm, 8u);
      const float mi = sm.wmax[(l >> 1) & 1][(l >> 2) & 7];
      float M = fmaxf(mi, rorf<4>(mi)); M = fmaxf(M, rorf<8>(M)); M = max16(M);
      fsc = __builtin_amdgcn_exp2f(mw - M);
      asm volatile("" : "+v"(fsc));
      FENCE();
    }
    PV(10); PV(11); PV(12); PV(13); PV(14); PV(15);
    {
      float* ap = &sm.acc[w - 8][(((l >> 1) & 1) * 8 + 2 * (l >> 4)) * 16 + (bank * 2 + (l & 1)) * 2];
      *(f32x2*)ap = f32x2{acc[0] * fsc, acc[1] * fsc};
      *(f32x2*)(ap + 16) = f32x2{acc[2] * fsc, acc[3] * fsc};
      asm volatile("" ::: "memory");
      BUMP(ca);
      if (w == 8) STAMP(11);
      WST(6);
      FENCE();
    }
  }
  FENCE();
  float y[8];
  if (ow) {
    const int rs = l >> 3;
    u32 h0, h1, h2;
    for (u32 it = 0;; ++it) {
      h0 = lds_ld(&sm.hv[rs]); h1 = lds_ld(&sm.hv[8 + rs]); h2 = lds_ld(&sm.hv[16 + rs]);
      if (__all(h0 != SENT && h1 != SENT && h2 != SENT) || it >= SPIN_L) break;
      __builtin_amdgcn_s_sleep(1);
    }
#pragma unroll
    for (int e = 0; e < 4; ++e) {
      y[2 * e] = __uint_as_float(h0) * bflo(dw[0][e]) + __uint_as_float(h1) * bflo(dw[1][e]) + __uint_as_float(h2) * bflo(dw[2][e]);
      y[2 * e + 1] = __uint_as_float(h0) * bfhi(dw[0][e]) + __uint_as_float(h1) * bfhi(dw[1][e]) + __uint_as_float(h2) * bfhi(dw[2][e]);
    }
    if (w == 0) STAMP(13);
  }
  FENCE();
  if (!ow) {
    const int p0 = w - 9;
    u32 wb_;
    for (u32 it = 0;; ++it) {
      wb_ = lds_ld(&sm.wgt[(p0 + 7 * (l >> 4)) & 15]);
      if (__all(wb_ != SENT) || it >= SPIN_L) break;
      __builtin_amdgcn_s_sleep(1);
    }
    const unsigned char* op = ws + WS_OX + (u32)u * 1024u + (u32)p0 * 64u;
    u32 pend = p0 < 2 ? 7u : 3u;
    for (u32 it = 0; it < SPIN_G && pend; ++it) {
      u32x16 v[3];
      if (pend & 1u) asm volatile("s_load_dwordx16 %0, %1, 0x0 glc" : "=&s"(v[0]) : "s"(op) : "memory");
      if (pend & 2u) asm volatile("s_load_dwordx16 %0, %1, 0x1c0 glc" : "=&s"(v[1]) : "s"(op) : "memory");
      if (pend & 4u) asm volatile("s_load_dwordx16 %0, %1, 0x380 glc" : "=&s"(v[2]) : "s"(op) : "memory");
      asm volatile("s_waitcnt lgkmcnt(0)" ::: "memory");
#pragma unroll
      for (int k = 0; k < 3; ++k) {
        if (pend & (1u << k)) {
          asm volatile("" : "+s"(v[k]));
          u32 mx = v[k][0];
#pragma unroll
          for (int e = 1; e < 16; ++e) mx = mx > v[k][e] ? mx : v[k][e];
          if (mx != SENT) {
            pend &= ~(1u << k);
            u32 t = 0;
#pragma unroll
            for (int e = 0; e < 16; ++e) asm("v_writelane_b32 %0, %1, %2" : "+v"(t) : "s"(v[k][e]), "n"(16 * k + e));
            const u32 r = nosent(__uint_as_float(t) * __uint_as_float(wb_));
            const int p = p0 + 7 * k;
            if ((l >> 4) == k) lds_st(&sm.op[(((l >> 1) & 7) * 8 + (p >> 1)) * 4 + (l & 1) * 2 + (p & 1)], r);
          }
        }
      }
    }
    if (pend) { lds_st(&sm.op[4 * l], 0u); lds_st(&sm.op[4 * l + 1], 0u); lds_st(&sm.op[4 * l + 2], 0u); lds_st(&sm.op[4 * l + 3], 0u); }
    if (w == 9) STAMP(20);
    if (w == 13) STAMP(15);
  }
  if (ow) {
    float wf[16];
#pragma unroll
    for (int e = 0; e < 4; ++e) { wf[2 * e] = bflo(dw[3][e]); wf[2 * e + 1] = bfhi(dw[3][e]); wf[8 + 2 * e] = bflo(dw[4][e]); wf[9 + 2 * e] = bfhi(dw[4][e]); }
    if (w == 0) STAMP(21);
    u32x4 o4;
    for (u32 it = 0;; ++it) {
      o4 = lds_ld4(&sm.op[4 * l]);
      if (__all(o4[0] != SENT && o4[1] != SENT && o4[2] != SENT && o4[3] != SENT) || it >= SPIN_L) break;
    }
    float o0 = __uint_as_float(o4[0]) + __uint_as_float(o4[1]), o1 = __uint_as_float(o4[2]) + __uint_as_float(o4[3]);
    o0 += dpp<0xB1>(o0); o1 += dpp<0xB1>(o1);
    o0 += dpp<0x4E>(o0); o1 += dpp<0x4E>(o1);
    o0 += dpp<0x141>(o0); o1 += dpp<0x141>(o1);
#pragma unroll
    for (int e = 0; e < 8; ++e) y[e] += o0 * wf[e] + o1 * wf[8 + e];
    float a4[4], a2[2];
#pragma unroll
    for (int e = 0; e < 4; ++e) a4[e] = red32(y[e], y[e + 4]);
#pragma unroll
    for (int e = 0; e < 2; ++e) a2[e] = red16(a4[e], a4[e + 2]);
    const float t_ = (l & 8) ? a2[0] : a2[1], kp_ = (l & 8) ? a2[1] : a2[0];
    const float a1 = kp_ + dpp<0x128>(t_);
    __builtin_amdgcn_raw_buffer_store_b32(nosent(a1), rWS, WS_FX + (u32)(8 * w + (l & 7)) * 8192u + (u32)u * 32u +
                                          (4 * (l >> 5) + 2 * ((l >> 4) & 1) + ((l >> 3) & 1)) * 4, 0, AUX_SC1);
    if (w == 0) STAMP(14);
  }
  if (fcons) {
    const int pw = w & 7;
    if (w >= 9) {
      for (u32 it = 0; it < SPIN_L; ++it) {
        const u32x4 o4 = lds_ld4(&sm.op[4 * l]);
        if (__all(o4[0] != SENT && o4[1] != SENT && o4[2] != SENT && o4[3] != SENT)) break;
        __builtin_amdgcn_s_sleep(2);
      }
    }
#define FDELAY 4
    __builtin_amdgcn_s_sleep(FDELAY);
    if (w >= 8) __builtin_amdgcn_s_sleep(5);
    const u32 xo = *((lds_u16*)sm.xs + cidx * 8 + (l & 7));
    const u32 fb = WS_FX + cidx * 8192u + pw * 1024 + l * 16;
    u32x4 t;
    bool have = false;
    for (u32 it = 0; it < SPIN_G; ++it) {
      asm volatile("" ::: "memory");
      t = __builtin_amdgcn_raw_buffer_load_b128(rWS, fb, 0, AUX_SC1);
      if (__all(t[0] != SENT && t[1] != SENT && t[2] != SENT && t[3] != SENT)) { have = true; break; }
      if (lds_ld(&sm.fin[pw * 8]) != SENT) break;
    }
    if (have) {
      const float b0 = red32(__uint_as_float(t[0]), __uint_as_float(t[2])), b1 = red32(__uint_as_float(t[1]), __uint_as_float(t[3]));
      float c = red16(b0, b1);
      c += dpp<0x4E>(c); c += dpp<0x124>(c); c += dpp<0x128>(c);
      if ((l & 14) == 0) lds_st(&sm.fin[pw * 8 + 4 * (l & 1) + 2 * (l >> 5) + ((l >> 4) & 1)], nosent(c));
    }
    if (w == 0) STAMP(22);
    u32 fv;
    for (u32 it = 0;; ++it) {
      fv = lds_ld(&sm.fin[l]);
      if (__all(fv != SENT) || it >= SPIN_L) break;
    }
    float c = __uint_as_float(fv);
    c += dpp<0x128>(c); c = red16(c, c); c = red32(c, c);
    const __bf16 r = (__bf16)(bflo(xo) + c);
    if (l < 8) out[cidx * 8 + l] = __builtin_bit_cast(unsigned short, r);
    if (cidx == 0 && l == 0) __builtin_amdgcn_raw_buffer_store_b32(sm.ev + 1u, rWS, WS_E, 0, AUX_SC1);
    if (w == 0) STAMP(23);
  }
}

extern "C" void kernel_launch(void* const* d_in, const int* in_sizes, int n_in, void* d_out, int out_size, void* d_ws, size_t ws_size,
                              hipStream_t stream) {
  fused_decode<<<256, 1024, 0, stream>>>((const unsigned short*)d_in[0], (const unsigned short*)d_in[1], (const unsigned short*)d_in[2],
                                         (const unsigned short*)d_in[3], (const unsigned short*)d_in[4], (const unsigned short*)d_in[6],
                                         (const unsigned short*)d_in[7], (const unsigned short*)d_in[8], (unsigned char*)d_ws,
                                         (unsigned short*)d_out);
}
```

```cpp
#include <hip/hip_runtime.h>
#include <hip/hip_bf16.h>
#include <cstdint>

typedef unsigned u32;
typedef u32 u32x4 __attribute__((ext_vector_type(4)));
typedef u32 u32x2 __attribute__((ext_vector_type(2)));
typedef u32 u32x16 __attribute__((ext_vector_type(16)));
typedef float f32x4 __attribute__((ext_vector_type(4)));
typedef float f32x2 __attribute__((ext_vector_type(2)));
typedef __bf16 bf16x2 __attribute__((ext_vector_type(2)));
typedef __bf16 bf16x8 __attribute__((ext_vector_type(8)));
typedef __amdgpu_buffer_rsrc_t rsrc_t;

#define WIN 3

constexpr u32 WS_E = 0;
constexpr u32 WS_QX = 4096;
constexpr u32 WS_HX = WS_QX + 256 * 512;
constexpr u32 WS_FL = WS_HX + 256 * 1152;
constexpr u32 WS_AG = WS_FL + 256 * 128;
constexpr u32 WS_ML = WS_AG + 16 * 512;
constexpr u32 WS_OX = WS_ML + 256 * 128;
constexpr u32 WS_FX = WS_OX + 256 * 1024;
constexpr u32 WS_END = WS_FX + 72 * 8192;
constexpr u32 SPIN_G = 40000;
constexpr u32 SPIN_L = 4000000;
constexpr u32 SENT = 0xFFFFFFFFu;
constexpr u32 OOR = 0xFFF00000u;

#define STAMP(i) do { } while (0)
#define WST(id) do { } while (0)

#define AUX_NT 2
#define AUX_SC1 16
#define FENCE() __builtin_amdgcn_sched_barrier(0)
#define THR(n) do { asm volatile("s_waitcnt vmcnt(%0)" :: "n"(n) : "memory"); FENCE(); } while (0)
#define THROTTLE() THR(WIN - 1)
#define LDSWAIT() asm volatile("s_waitcnt lgkmcnt(0)" ::: "memory")

__device__ __forceinline__ float bflo(u32 u) { return __uint_as_float(u << 16); }
__device__ __forceinline__ float bfhi(u32 u) { return __uint_as_float(u & 0xffff0000u); }
template <int CTRL> __device__ __forceinline__ float dpp(float x) {
  return __int_as_float(__builtin_amdgcn_update_dpp(0, __float_as_int(x), CTRL, 0xf, 0xf, true));
}
template <int N> __device__ __forceinline__ float rorf(float x) {
  if constexpr ((N & 15) == 0) return x; else return dpp<0x120 + (N & 15)>(x);
}
template <int N> __device__ __forceinline__ u32 roru(u32 x) {
  if constexpr ((N & 15) == 0) return x; else return (u32)__builtin_amdgcn_update_dpp(0, (int)x, 0x120 + (N & 15), 0xf, 0xf, true);
}
__device__ __forceinline__ float red32(float a, float b) {
  auto r = __builtin_amdgcn_permlane32_swap(__float_as_uint(a), __float_as_uint(b), false, false);
  return __uint_as_float(r[0]) + __uint_as_float(r[1]);
}
__device__ __forceinline__ float red16(float a, float b) {
  auto r = __builtin_amdgcn_permlane16_swap(__float_as_uint(a), __float_as_uint(b), false, false);
  return __uint_as_float(r[0]) + __uint_as_float(r[1]);
}
__device__ __forceinline__ float max32(float a) {
  auto r = __builtin_amdgcn_permlane32_swap(__float_as_uint(a), __float_as_uint(a), false, false);
  return fmaxf(__uint_as_float(r[0]), __uint_as_float(r[1]));
}
__device__ __forceinline__ float max16(float a) {
  auto r = __builtin_amdgcn_permlane16_swap(__float_as_uint(a), __float_as_uint(a), false, false);
  return fmaxf(__uint_as_float(r[0]), __uint_as_float(r[1]));
}
__device__ __forceinline__ float rowsum16(float s) {
  s += dpp<0xB1>(s); s += dpp<0x4E>(s); s += dpp<0x141>(s); s += dpp<0x140>(s);
  return s;
}
__device__ __forceinline__ float rowmax16(float s) {
  s = fmaxf(s, dpp<0xB1>(s)); s = fmaxf(s, dpp<0x4E>(s)); s = fmaxf(s, dpp<0x141>(s)); s = fmaxf(s, dpp<0x140>(s));
  return s;
}
__device__ __forceinline__ float wavesum(float s) { s = rowsum16(s); s = red16(s, s); s = red32(s, s); return s; }
__device__ __forceinline__ rsrc_t mkr(const void* p, u32 n) { return __builtin_amdgcn_make_buffer_rsrc((void*)p, 0, n, 0x00020000); }
__device__ __forceinline__ u32x4 gran(float a, float b, u32 tag) { u32x4 r = {__float_as_uint(a), tag, __float_as_uint(b), tag}; return r; }
__device__ __forceinline__ bool tagok(u32x4 v, u32 tag) { return v[1] == tag && v[3] == tag; }
typedef __attribute__((address_space(3))) volatile u32 lds_u32;
typedef __attribute__((address_space(3))) volatile u32x4 lds_u32x4;
typedef __attribute__((address_space(3))) volatile unsigned short lds_u16;
__device__ __forceinline__ u32 lds_ld(u32* p) { return *(lds_u32*)p; }
__device__ __forceinline__ u32x4 lds_ld4(u32* p) { return *(lds_u32x4*)p; }
__device__ __forceinline__ void lds_st(u32* p, u32 v) { *(lds_u32*)p = v; }
#define PIN8(a) asm volatile("" : "+v"(a[0]), "+v"(a[1]), "+v"(a[2]), "+v"(a[3]), "+v"(a[4]), "+v"(a[5]), "+v"(a[6]), "+v"(a[7]))
__device__ __forceinline__ u32 nosent(float v) { const u32 t = __float_as_uint(v); return t == SENT ? 0x7FC00000u : t; }
__device__ __forceinline__ u32 arrive(u32* c, int l) {
  u32 old = 0;
  LDSWAIT();
  if (l == 0) old = __hip_atomic_fetch_add(c, 1u, __ATOMIC_RELAXED, __HIP_MEMORY_SCOPE_WORKGROUP);
  old = __builtin_amdgcn_readfirstlane(old);
  asm volatile("" ::: "memory");
  return old;
}
__device__ __forceinline__ f32x4 mfma(u32x4 a, u32x4 b, f32x4 c) {
  return __builtin_amdgcn_mfma_f32_16x16x32_bf16(__builtin_bit_cast(bf16x8, a), __builtin_bit_cast(bf16x8, b), c, 0, 0, 0);
}

struct __align__(16) Smem {
  float qp[8][64];
  float hp[36][144];
  float acc[8][256];
  float wmax[2][8];
  float wl[2][8];
  u32 qv[2][2][64];
  u32 xs[320];
  float xn[640];
  u32 op[256];
  u32 fin[64];
  u32 hv[24];
  u32 wgt[16];
  u32 cq, ch, cm, ca, fq, fx, cv, ff;
  u32 ev, t0, pad4, pad5;
};
#define WA 3
#define NA 3
#define WB 6
#define NB 32
#define WC 6
#define NC 32
#define WD 6
#define BARI 2
#define TRIGN 8u
#define QSLP 4
#define HSLP 8
#define AUX_1ST 0
#define MSLP 4
#define MSLP0 16
#define AUX_E 0
#define WS_ 3
#define QPOS 7
#define TRIG 15
#define PACE_AT(base, T) do {                                                                                               \
      asm volatile("1: s_memrealtime s[94:95]\n s_waitcnt lgkmcnt(0)\n s_sub_u32 s94, s94, %0\n s_cmpk_lt_i32 s94, %1\n"    \
                   " s_cbranch_scc0 2f\n s_sleep 1\n s_branch 1b\n2:"                                                      \
                   :: "s"(base), "n"(T) : "s94", "s95", "scc", "memory");                                                   \
      FENCE(); } while (0)
#define PN0 3
#define PT0 2150
#define N1 4
#define N2 17
#define TAU1 285
#define TAU2 380
#define TAU3 285
constexpr int TK(int n) { return n <= N1 ? PT0 + (n - PN0) * TAU1 : n <= N2 ? TK(N1) + (n - N1) * TAU2 : TK(N2) + (n - N2) * TAU3; }
#define GT0 2600
#define GTAU 700
#define DOFF 500
#define DTAU 280
#define DSTAG 0
#define BUMP(c) do { if (l == 0) __hip_atomic_fetch_add(&sm.c, 1u, __ATOMIC_RELAXED, __HIP_MEMORY_SCOPE_WORKGROUP); } while (0)
#define LWAIT(c, n) do { for (u32 it = 0; lds_ld(&sm.c) != (n) && it < SPIN_L; ++it) __builtin_amdgcn_s_sleep(1); asm volatile("" ::: "memory"); } while (0)

__global__ void __launch_bounds__(1024, 4)
fused_decode(const unsigned short* __restrict__ x, const unsigned short* __restrict__ Kc, const unsigned short* __restrict__ Vc,
             const unsigned short* __restrict__ wq, const unsigned short* __restrict__ wo, const unsigned short* __restrict__ wg,
             const unsigned short* __restrict__ wu, const unsigned short* __restrict__ wd, unsigned char* __restrict__ ws,
             unsigned short* __restrict__ out) {
  __shared__ Smem sm;
  const int b = blockIdx.x;
  const int w = __builtin_amdgcn_readfirstlane(threadIdx.x >> 6);
  const int l = threadIdx.x & 63;
#define PXA 0
#define PXB 0
  const int u = ((b + PXA) & 7) * 32 + ((b >> 3) ^ PXB);
  const int g = u >> 4, j = u & 15, k4 = u & 3;
  const bool fcons = (u & 31) < 9;
  const u32 cidx = (u32)((u >> 5) * 9 + (u & 31));
  const u32 tb = (u32)__builtin_amdgcn_s_memrealtime();
  asm volatile("" :: "s"(x), "s"(Kc), "s"(Vc), "s"(wq), "s"(wo), "s"(wg), "s"(wu), "s"(wd), "s"(ws));
  const rsrc_t rWS = mkr(ws, WS_END);
  const bool ow = w < 9;
  const rsrc_t rD = mkr(wd, 4608u * 1152u), rO = mkr(wo, 4096u * 1152u);
  const u32 co = (u32)w * 128u + (l & 7) * 16;
  const u32 od = ow ? (u32)(18 * u + (l >> 3)) * 1152u + co : OOR;
  const u32 oo = ow ? (u32)(16 * u + (l >> 3)) * 1152u + co : OOR;
  u32x4 dw[5];
#define DWLOADS() do {                                                                                     \
    dw[0] = __builtin_amdgcn_raw_buffer_load_b128(rD, od, 0, AUX_NT);                                      \
    dw[1] = __builtin_amdgcn_raw_buffer_load_b128(rD, od, 8 * 1152, AUX_NT);                               \
    dw[2] = __builtin_amdgcn_raw_buffer_load_b128(rD, l < 16 ? od : OOR, 16 * 1152, AUX_NT);               \
    dw[3] = __builtin_amdgcn_raw_buffer_load_b128(rO, oo, 0, AUX_NT);                                      \
    dw[4] = __builtin_amdgcn_raw_buffer_load_b128(rO, oo, 8 * 1152, AUX_NT); FENCE(); } while (0)

  if (w < 8) {
    const u32 ts0 = tb + (u32)(w * (GTAU / 60)), td0 = tb + (u32)(w * (DSTAG / 10));
    const rsrc_t rX = mkr(x, 1152), rQ = mkr(wq, 576u * 8192u), rG = mkr(wg, 576u * 9216u), rU = mkr(wu, 576u * 9216u);
    const int s6 = l / 9, c9 = l - 9 * s6;
    const int rb = 144 * k4 + 18 * w, rg = 144 * k4 + 24 * (w < 6 ? w : 0);
    const u32 qo = (u32)(rb + (l >> 3)) * 8192u + (u32)(256 * g + 64 * (j >> 2)) * 2 + (l & 7) * 16;
    const u32 go = l < 54 && w < 6 ? (u32)(rg + s6) * 9216u + (u32)(u >> 2) * 144u + c9 * 16 : OOR;
#define AUX_G AUX_NT
    u32x4 xa = {0u, 0u, 0u, 0u};
    u32 xb = 0u, Ev = 0u;
    if (w == 0) {
      xa = __builtin_amdgcn_raw_buffer_load_b128(rX, l * 16, 0, 0);
      xb = __builtin_amdgcn_raw_buffer_load_b32(rX, 1024 + l * 4, 0, 0);
      Ev = __builtin_amdgcn_raw_buffer_load_b32(rWS, WS_E, 0, AUX_E);
    }
    {
      const u32x4 sv = {SENT, SENT, SENT, SENT};
      if (w == 7) __builtin_amdgcn_raw_buffer_store_b128(sv, rWS, WS_OX + (u32)u * 1024u + l * 16, 0, AUX_SC1);
      if (w == 6) __builtin_amdgcn_raw_buffer_store_b128(sv, rWS, l < 8 ? WS_ML + (u32)u * 128u + l * 16 : OOR, 0, AUX_SC1);
      if (fcons) __builtin_amdgcn_raw_buffer_store_b128(sv, rWS, WS_FX + cidx * 8192u + (u32)w * 1024u + l * 16, 0, AUX_SC1);
    }
    FENCE();
    u32x4 fq_[3], fg[4], fu[4];
    fq_[0] = __builtin_amdgcn_raw_buffer_load_b128(rQ, qo, 0, AUX_NT); FENCE();
    fq_[1] = __builtin_amdgcn_raw_buffer_load_b128(rQ, qo, 8 * 8192, AUX_NT); FENCE();
    fq_[2] = __builtin_amdgcn_raw_buffer_load_b128(rQ, l < 16 ? qo : OOR, 16 * 8192, AUX_NT); FENCE();
    if (threadIdx.x < 368) sm.op[threadIdx.x] = threadIdx.x < 360 ? SENT : 0u;
    LDSWAIT(); __builtin_amdgcn_s_barrier(); FENCE(); WST(0);
    fg[0] = __builtin_amdgcn_raw_buffer_load_b128(rG, go, 0, AUX_G); FENCE();
    if (w == 0) {
      float xf[10];
#pragma unroll
      for (int e = 0; e < 4; ++e) { xf[2 * e] = bflo(xa[e]); xf[2 * e + 1] = bfhi(xa[e]); }
      xf[8] = bflo(xb); xf[9] = bfhi(xb);
      const float mean = wavesum((((xf[0] + xf[1]) + (xf[2] + xf[3])) + ((xf[4] + xf[5]) + (xf[6] + xf[7]))) + (xf[8] + xf[9])) * (1.0f / 576.0f);
#pragma unroll
      for (int e = 0; e < 10; ++e) xf[e] -= mean;
      float v0 = xf[0] * xf[0], v1 = xf[1] * xf[1];
#pragma unroll
      for (int e = 2; e < 8; e += 2) { v0 += xf[e] * xf[e]; v1 += xf[e + 1] * xf[e + 1]; }
      v0 += l < 32 ? xf[8] * xf[8] + xf[9] * xf[9] : 0.f;
      const float rstd = __builtin_amdgcn_rsqf(wavesum(v0 + v1) * (1.0f / 576.0f) + 1e-5f);
      *(f32x4*)&sm.xn[8 * l] = f32x4{xf[0] * rstd, xf[1] * rstd, xf[2] * rstd, xf[3] * rstd};
      *(f32x4*)&sm.xn[8 * l + 4] = f32x4{xf[4] * rstd, xf[5] * rstd, xf[6] * rstd, xf[7] * rstd};
      *(f32x2*)&sm.xn[512 + 2 * l] = f32x2{xf[8] * rstd, xf[9] * rstd};
      *(u32x4*)&sm.xs[4 * l] = xa;
      sm.xs[256 + l] = xb;
      if (l == 0) sm.ev = Ev;
      LDSWAIT();
      if (l == 0) lds_st(&sm.fx, 1u);
      STAMP(2);
    }
    FENCE();
    LWAIT(fx, 1u);
    const u32 tag = __builtin_amdgcn_readfirstlane(sm.ev) * 0x9E3779B1u + 0x7F4A7C15u;
    float xq[3], xg[4];
#pragma unroll
    for (int i = 0; i < 3; ++i) xq[i] = sm.xn[rb + (l >> 3) + 8 * i];
#pragma unroll
    for (int i = 0; i < 4; ++i) xg[i] = sm.xn[rg + (l < 54 ? s6 : 0) + 6 * i];
    asm volatile("" : "+v"(xq[0]), "+v"(xq[1]), "+v"(xq[2]), "+v"(xg[0]), "+v"(xg[1]), "+v"(xg[2]), "+v"(xg[3]));
    FENCE();
    THR(1);
    WST(1);
#define FACC(N, G, F, X) do {                                                                                          \
      _Pragma("unroll") for (int e = 0; e < 4; ++e) G[e] = f32x2{X[0], X[0]} * f32x2{bflo(F[0][e]), bfhi(F[0][e])};    \
      _Pragma("unroll") for (int i = 1; i < N; ++i)                                                                    \
        _Pragma("unroll") for (int e = 0; e < 4; ++e) G[e] += f32x2{X[i], X[i]} * f32x2{bflo(F[i][e]), bfhi(F[i][e])}; \
    } while (0)
    {
      f32x2 g2[4];
      FACC(3, g2, fq_, xq);
      float a4[4], a2[2];
#pragma unroll
      for (int e = 0; e < 4; ++e) a4[e] = red32(g2[e >> 1][e & 1], g2[2 + (e >> 1)][e & 1]);
#pragma unroll
      for (int e = 0; e < 2; ++e) a2[e] = red16(a4[e], a4[e + 2]);
      const float t_ = (l & 8) ? a2[0] : a2[1], kp_ = (l & 8) ? a2[1] : a2[0];
      sm.qp[w][8 * (l & 7) + 4 * (l >> 5) + 2 * ((l >> 4) & 1) + ((l >> 3) & 1)] = kp_ + dpp<0x128>(t_);
      if (w == 0) STAMP(1);
      if (arrive(&sm.cq, l) == 7u) {
        const int P_ = l & 31, hf = l >> 5;
        const f32x2 s_ = (*(const f32x2*)&sm.qp[hf * 4][2 * P_] + *(const f32x2*)&sm.qp[hf * 4 + 1][2 * P_]) +
                         (*(const f32x2*)&sm.qp[hf * 4 + 2][2 * P_] + *(const f32x2*)&sm.qp[hf * 4 + 3][2 * P_]);
        const float s0 = red32(s_[0], s_[0]), s1 = red32(s_[1], s_[1]);
        if (l < 32) __builtin_amdgcn_raw_buffer_store_b128(gran(s0, s1, tag), rWS, WS_QX + (u32)u * 512u + P_ * 16, 0, AUX_SC1);
        const u32 fa = l < 16 ? WS_FL + (u32)(g * 16 + l) * 128u + j * 4 : (fcons && l < 32) ? WS_AG + (u32)(l - 16) * 512u + cidx * 4u : OOR;
        __builtin_amdgcn_raw_buffer_store_b32(tag, rWS, fa, 0, AUX_SC1);
        STAMP(3);
      }
      FENCE();
    }
    if (w < 6) {
      float* const hp = &sm.hp[w * 6 + (l < 54 ? s6 : 0)][8 * c9];
#define GLD(k, d, r, ro) do { PACE_AT(ts0, (GT0 + ((k) - 1) * GTAU) / 10); d = __builtin_amdgcn_raw_buffer_load_b128(r, go, (ro) * 9216, AUX_G); FENCE(); } while (0)
      GLD(1, fg[1], rG, 6); GLD(2, fg[2], rG, 12); GLD(3, fg[3], rG, 18);
      GLD(4, fu[0], rU, 0); GLD(5, fu[1], rU, 6); GLD(6, fu[2], rU, 12); GLD(7, fu[3], rU, 18);
      f32x2 g2[4], u2[4];
      FACC(4, g2, fg, xg);
      if (l < 54) {
        *(f32x4*)hp = f32x4{g2[0][0], g2[0][1], g2[1][0], g2[1][1]};
        *(f32x4*)(hp + 4) = f32x4{g2[2][0], g2[2][1], g2[3][0], g2[3][1]};
      }
      FENCE();
      FACC(4, u2, fu, xg);
      if (l < 54) {
        *(f32x4*)(hp + 72) = f32x4{u2[0][0], u2[0][1], u2[1][0], u2[1][1]};
        *(f32x4*)(hp + 76) = f32x4{u2[2][0], u2[2][1], u2[3][0], u2[3][1]};
      }
      LDSWAIT();
      BUMP(ch);
      WST(2);
      if (w < 4) {
        LWAIT(ch, 6u);
        const int gi = l & 31, v = gi < 18 ? gi : 0, hf = l >> 5;
        const int col = (v >= 9 ? 72 + 2 * (v - 9) : 2 * v) + 18 * w;
        f32x2 sa = {0.f, 0.f}, sb = {0.f, 0.f};
#pragma unroll
        for (int i = 0; i < 18; i += 2) { sa += *(const f32x2*)&sm.hp[hf * 18 + i][col]; sb += *(const f32x2*)&sm.hp[hf * 18 + i + 1][col]; }
        sa += sb;
        const float s0 = red32(sa[0], sa[0]), s1 = red32(sa[1], sa[1]);
        if (l < 18) __builtin_amdgcn_raw_buffer_store_b128(gran(s0, s1, tag), rWS, WS_HX + (u32)((u >> 2) * 4 + w) * 1152u + k4 * 288 + l * 16, 0, AUX_SC1);
        if (w == 0) STAMP(6);
      }
    } else {
      if (w == 6) {
        const unsigned char* fp = ws + WS_FL + (u32)u * 128u;
        LWAIT(cq, 8u);
        for (u32 it = 0; it < SPIN_G; ++it) {
          u32x16 v;
          __builtin_amdgcn_s_sleep(QSLP);
          asm volatile("s_load_dwordx16 %0, %1, 0x0 glc\n s_waitcnt lgkmcnt(0)" : "=&s"(v) : "s"(fp) : "memory");
          u32 bad = 0;
#pragma unroll
          for (int e = 0; e < 16; ++e) bad |= v[e] ^ tag;
          if (bad == 0) break;
        }
        if (l == 0) lds_st(&sm.ff, 1u);
      } else {
        LWAIT(ff, 1u);
      }
      if (w == 6) STAMP(4);
      const u32 g1a = WS_QX + (u32)(g * 16 + (w - 6) * 8 + (l >> 4)) * 512u + (l & 15) * 16;
      u32x4 g1[4];
#pragma unroll
      for (int i = 0; i < 4; ++i) g1[i] = __builtin_amdgcn_raw_buffer_load_b128(rWS, g1a + (i & 1) * 256 + (i >> 1) * 2048, 0, AUX_1ST);
      asm volatile("" : "+v"(g1[0]), "+v"(g1[1]), "+v"(g1[2]), "+v"(g1[3]));
      if (w == 6) STAMP(16);
      if (!__all(tagok(g1[0], tag) && tagok(g1[1], tag) && tagok(g1[2], tag) && tagok(g1[3], tag))) {
        for (u32 it = 0;; ++it) {
          asm volatile("" ::: "memory");
#pragma unroll
          for (int i = 0; i < 4; ++i) g1[i] = __builtin_amdgcn_raw_buffer_load_b128(rWS, g1a + (i & 1) * 256 + (i >> 1) * 2048, 0, AUX_SC1);
          asm volatile("" : "+v"(g1[0]), "+v"(g1[1]), "+v"(g1[2]), "+v"(g1[3]));
          if (__all(tagok(g1[0], tag) && tagok(g1[1], tag) && tagok(g1[2], tag) && tagok(g1[3], tag)) || it >= SPIN_G) break;
        }
      }
      const float qs = 0.08838834764831845f * 1.4426950408889634f;
#pragma unroll
      for (int i = 0; i < 4; ++i) {
        float a = red16(__uint_as_float(g1[i][0]), __uint_as_float(g1[i][2]));
        a = red32(a, a) * qs;
        const u32 hib = __builtin_bit_cast(unsigned short, (__bf16)a);
        const u32 lob = __builtin_bit_cast(unsigned short, (__bf16)(a - bflo(hib)));
        *((lds_u16*)&sm.qv[w - 6][l >> 5][16 * i + (l & 15)] + ((l >> 4) & 1)) = (unsigned short)(l < 32 ? hib : lob);
      }
      LDSWAIT();
      BUMP(fq);
      if (w == 6) STAMP(5);
      WST(3);
    }
    FENCE();
    if (w == 7) {
      const int v = (l & 31) < 18 ? (l & 31) : 0;
      const u32 hb = WS_HX + (u32)u * 1152u + (l >> 5) * 288 + v * 16;
      const unsigned char* hs = ws + WS_HX + (u32)u * 1152u;
      LWAIT(ch, 6u);
      for (u32 it = 0; it < SPIN_G; ++it) {
        u32 a0, a1, a2, a3;
        __builtin_amdgcn_s_sleep(HSLP);
        asm volatile("s_load_dword %0, %4, 0x11c glc\n s_load_dword %1, %4, 0x23c glc\n s_load_dword %2, %4, 0x35c glc\n s_load_dword %3, %4, 0x47c glc\n s_waitcnt lgkmcnt(0)"
                     : "=&s"(a0), "=&s"(a1), "=&s"(a2), "=&s"(a3) : "s"(hs) : "memory");
        if (((a0 ^ tag) | (a1 ^ tag) | (a2 ^ tag) | (a3 ^ tag)) == 0) break;
      }
      u32x4 t0, t1;
      for (u32 it = 0;; ++it) {
        asm volatile("" ::: "memory");
        const u32x4 e0 = __builtin_amdgcn_raw_buffer_load_b128(rWS, WS_AG + (u32)g * 512u + v * 16, 0, AUX_SC1);
        t0 = __builtin_amdgcn_raw_buffer_load_b128(rWS, hb, 0, AUX_SC1);
        t1 = __builtin_amdgcn_raw_buffer_load_b128(rWS, hb + 576, 0, AUX_SC1);
        if (__all(tagok(e0, tag) && e0[0] == tag && e0[2] == tag && tagok(t0, tag) && tagok(t1, tag)) || it >= SPIN_G) break;
        __builtin_amdgcn_s_sleep(8);
      }
      float c0 = __uint_as_float(t0[0]) + __uint_as_float(t1[0]), c1_ = __uint_as_float(t0[2]) + __uint_as_float(t1[2]);
      c0 = red32(c0, c0); c1_ = red32(c1_, c1_);
      const int src = (l < 18 ? l >> 1 : 0) * 4;
      const int g0 = __builtin_amdgcn_ds_bpermute(src, __float_as_int(c0)), g1 = __builtin_amdgcn_ds_bpermute(src, __float_as_int(c1_));
      const int u0 = __builtin_amdgcn_ds_bpermute(src + 36, __float_as_int(c0)), u1 = __builtin_amdgcn_ds_bpermute(src + 36, __float_as_int(c1_));
      const float gs = __int_as_float((l & 1) ? g1 : g0), us = __int_as_float((l & 1) ? u1 : u0);
      const float hval = l < 18 ? gs / (1.0f + __expf(-gs)) * us : 0.f;
      if (l < 24) lds_st(&sm.hv[l], nosent(hval));
      STAMP(10);
    } else if (w == 6) {
      LWAIT(cm, 8u);
      const int h = (l >> 3) & 1;
      const float mi = sm.wmax[h][l & 7], li = sm.wl[h][l & 7];
      float M = fmaxf(mi, dpp<0xB1>(mi)); M = fmaxf(M, dpp<0x4E>(M)); M = fmaxf(M, dpp<0x141>(M));
      float L = li * __builtin_amdgcn_exp2f(mi - M);
      L += dpp<0xB1>(L); L += dpp<0x4E>(L); L += dpp<0x141>(L);
      const u32x2 ml = {nosent(M), nosent(L)};
      __builtin_amdgcn_raw_buffer_store_b64(ml, rWS, l < 16 ? WS_ML + (u32)(g * 16 + l) * 128u + j * 8 : OOR, 0, AUX_SC1);
      STAMP(17);
      __builtin_amdgcn_s_sleep(MSLP0);
      const unsigned char* mp = ws + WS_ML + (u32)u * 128u;
      u32x16 v[2];
      for (u32 it = 0; it < SPIN_G; ++it) {
        asm volatile("s_load_dwordx16 %0, %1, 0x0 glc" : "=&s"(v[0]) : "s"(mp) : "memory");
        asm volatile("s_load_dwordx16 %0, %1, 0x40 glc" : "=&s"(v[1]) : "s"(mp) : "memory");
        asm volatile("s_waitcnt lgkmcnt(0)" ::: "memory");
        u32 mx = 0;
#pragma unroll
        for (int k = 0; k < 2; ++k) {
          asm volatile("" : "+s"(v[k]));
#pragma unroll
          for (int e = 0; e < 16; ++e) mx = mx > v[k][e] ? mx : v[k][e];
        }
        if (mx != SENT) break;
        __builtin_amdgcn_s_sleep(MSLP);
      }
      u32 tm = 0, tl = 0;
#pragma unroll
      for (int k = 0; k < 2; ++k)
#pragma unroll
        for (int e = 0; e < 8; ++e) {
          asm("v_writelane_b32 %0, %1, %2" : "+v"(tm) : "s"(v[k][2 * e]), "n"(8 * k + e));
          asm("v_writelane_b32 %0, %1, %2" : "+v"(tl) : "s"(v[k][2 * e + 1]), "n"(8 * k + e));
        }
      asm volatile("s_nop 1" : "+v"(tm), "+v"(tl));
      const float mi2 = __uint_as_float(tm);
      const float e = __builtin_amdgcn_exp2f(mi2 - rowmax16(mi2));
      const float L2 = rowsum16(e * __uint_as_float(tl));
      if (l < 16) lds_st(&sm.wgt[l], nosent(e / L2));
      STAMP(18);
    }
    FENCE();
    for (u32 it = 0; lds_ld(&sm.cv) < TRIGN && it < SPIN_L; ++it) __builtin_amdgcn_s_sleep(1);
    asm volatile("" ::: "memory");
    if (w == 0) STAMP(24);
#define DPACE(k) PACE_AT(td0, (TK(31) + DOFF + (k) * DTAU) / 10)
    DPACE(0); dw[0] = __builtin_amdgcn_raw_buffer_load_b128(rD, od, 0, AUX_NT); FENCE();
    DPACE(1); dw[1] = __builtin_amdgcn_raw_buffer_load_b128(rD, od, 8 * 1152, AUX_NT); FENCE();
    DPACE(2); dw[3] = __builtin_amdgcn_raw_buffer_load_b128(rO, oo, 0, AUX_NT); FENCE();
    DPACE(3); dw[4] = __builtin_amdgcn_raw_buffer_load_b128(rO, oo, 8 * 1152, AUX_NT); FENCE();
    dw[2] = __builtin_amdgcn_raw_buffer_load_b128(rD, l < 16 ? od : OOR, 16 * 1152, AUX_NT); FENCE();
    if (w == 0) STAMP(7);
    FENCE();
  } else {
    const rsrc_t rK = mkr(Kc, 33554432u), rV = mkr(Vc, 33554432u);
#define PKV 0
#define KVI 0
    const u32 kvb = (u32)(u ^ PKV) * 131072u + (u32)(w - 8) * 16384u;
#define TVO(i) (((i) & 3) * 1024)
#define TSO(i) (((i) >> 2) * 4096)
    const u32 voff = kvb + (u32)l * 16u;
    const u32 koff = kvb + (u32)(((l >> 2) & 3) * 256 + (l >> 4) * 64 + (l & 3) * 16);
    u32x4 kk[16], vv[16];
#define WINOF(n) ((n) < NA ? WA : (n) < NB ? WB : (n) < NC ? WC : WD)
#define PACE(n) do { if ((n) >= PN0) PACE_AT(tb, TK(n) / 10); } while (0)
#define KLD(i) do { if ((i) >= WA) THR(WINOF(i) - 1);                                                                      \
      PACE(i);                                                                                                              \
      kk[i] = __builtin_amdgcn_raw_buffer_load_b128(rK, koff + TVO(i), TSO(i), AUX_NT); FENCE();                            \
      if ((i) == BARI) { __builtin_amdgcn_s_barrier(); FENCE(); WST(0); } } while (0)
#define VLD(i) do { THR(WINOF(16 + (i)) - 1);                                                                               \
      PACE(16 + (i));                                                                                                       \
      vv[i] = __builtin_amdgcn_raw_buffer_load_b128(rV, voff + TVO(i), TSO(i), AUX_NT); FENCE();                            \
      if ((i) == TRIG) { BUMP(cv); FENCE(); } } while (0)
    KLD(0); KLD(1); KLD(2); KLD(3); KLD(4); KLD(5); KLD(6); KLD(7);
    WST(1);
    KLD(8); KLD(9); KLD(10); KLD(11); KLD(12); KLD(13); KLD(14); KLD(15);
    WST(2);
    VLD(0); VLD(1); VLD(2); VLD(3); VLD(4); VLD(5);
    if (QPOS >= 6) VLD(6);
    if (QPOS >= 7) VLD(7);
    WST(3);
    LWAIT(fq, 2u);
    if (w == 8) STAMP(8);
    WST(4);
    const int bank = (l >> 2) & 3;
    u32 pk[4];
    float mw;
    {
      const u32x4 qb = *(const u32x4*)&sm.qv[(l >> 1) & 1][l & 1][4 * ((l >> 4) * 4 + bank)];
      float sc[4];
#pragma unroll
      for (int ab = 0; ab < 4; ++ab) {
        const f32x4 z = {0.f, 0.f, 0.f, 0.f};
        const f32x4 d0 = mfma(kk[4 * ab], qb, z), d1 = mfma(kk[4 * ab + 1], qb, z), d2 = mfma(kk[4 * ab + 2], qb, z), d3 = mfma(kk[4 * ab + 3], qb, z);
        float a0 = ((d0[0] + rorf<-4>(d0[1])) + rorf<-8>(d0[2])) + rorf<-12>(d0[3]);
        float a1 = ((d1[1] + rorf<4>(d1[0])) + rorf<-4>(d1[2])) + rorf<-8>(d1[3]);
        float a2 = ((d2[2] + rorf<8>(d2[0])) + rorf<4>(d2[1])) + rorf<-4>(d2[3]);
        float a3 = ((d3[3] + rorf<12>(d3[0])) + rorf<8>(d3[1])) + rorf<4>(d3[2]);
        asm volatile("" : "+v"(a0), "+v"(a1), "+v"(a2), "+v"(a3));
        const float t01 = (l & 4) ? a1 : a0, t23 = (l & 4) ? a3 : a2;
        const float t = (l & 8) ? t23 : t01;
        sc[ab] = t + dpp<0xB1>(t);
        asm volatile("" : "+v"(sc[ab]));
        if (ab == 1) { FENCE(); VLD(QPOS + 1); }
      }
      FENCE(); VLD(QPOS + 2);
      float m = fmaxf(fmaxf(sc[0], sc[1]), fmaxf(sc[2], sc[3]));
      m = fmaxf(m, rorf<4>(m)); m = fmaxf(m, rorf<8>(m));
      m = max32(max16(m));
      mw = m;
#pragma unroll
      for (int ab = 0; ab < 4; ++ab) {
        const float p = __builtin_amdgcn_exp2f(sc[ab] - m);
        const bf16x2 t = {(__bf16)p, (__bf16)p};
        pk[ab] = __builtin_bit_cast(u32, t);
      }
      float lt = (bfhi(pk[0]) + bfhi(pk[1])) + (bfhi(pk[2]) + bfhi(pk[3]));
      lt += rorf<4>(lt); lt += rorf<8>(lt);
      lt = red16(lt, lt); lt = red32(lt, lt);
      if ((l & 61) == 0) { sm.wmax[l >> 1][w - 8] = mw; sm.wl[l >> 1][w - 8] = lt; }
      LDSWAIT();
      BUMP(cm);
    }
    if (w == 8) STAMP(9);
    WST(5);
    FENCE();
    const u32 hm = (l & 1) ? 0xffff0000u : 0x0000ffffu;
    const u32 m0 = bank == 0 ? hm : 0u, m1 = bank == 1 ? hm : 0u, m2 = bank == 2 ? hm : 0u, m3 = bank == 3 ? hm : 0u;
    u32x4 pb[16];
#define PBSET(t) do { pb[t][0] = roru<4 * (0 - ((t) & 3))>(pk[(t) >> 2]) & m0; pb[t][1] = roru<4 * (1 - ((t) & 3))>(pk[(t) >> 2]) & m1;   \
                      pb[t][2] = roru<4 * (2 - ((t) & 3))>(pk[(t) >> 2]) & m2; pb[t][3] = roru<4 * (3 - ((t) & 3))>(pk[(t) >> 2]) & m3;   \
                      asm volatile("" : "+v"(pb[t])); } while (0)
    f32x4 acc = {0.f, 0.f, 0.f, 0.f};
#define PV(i) do { acc = mfma(vv[i], pb[i], acc); asm volatile("" : "+v"(acc)); FENCE(); } while (0)
    if (QPOS < 6) VLD(8);
    if (QPOS < 7) VLD(9);
    PBSET(0); PBSET(1); PBSET(2); PBSET(3); PV(0); PV(1); PV(2); PV(3);
    VLD(10);
    PBSET(4); PBSET(5); PBSET(6); PBSET(7); PV(4);
    VLD(11);
    PV(5); PBSET(8); PBSET(9); PBSET(10); PBSET(11); FENCE();
    VLD(12);
    PV(6); PBSET(12); PBSET(13); PBSET(14); PBSET(15); FENCE();
    VLD(13);
    PV(7);
    VLD(14);
    PV(8);
    VLD(15);
    PV(9);
    float fsc;
    {
      LWAIT(cm, 8u);
      const float mi = sm.wmax[(l >> 1) & 1][(l >> 2) & 7];
      float M = fmaxf(mi, rorf<4>(mi)); M = fmaxf(M, rorf<8>(M)); M = max16(M);
      fsc = __builtin_amdgcn_exp2f(mw - M);
      asm volatile("" : "+v"(fsc));
      FENCE();
    }
    PV(10); PV(11); PV(12); PV(13); PV(14); PV(15);
    {
      float* ap = &sm.acc[w - 8][(((l >> 1) & 1) * 8 + 2 * (l >> 4)) * 16 + (bank * 2 + (l & 1)) * 2];
      *(f32x2*)ap = f32x2{acc[0] * fsc, acc[1] * fsc};
      *(f32x2*)(ap + 16) = f32x2{acc[2] * fsc, acc[3] * fsc};
      if (w == 8) STAMP(11);
      if (w == 15) STAMP(25);
      WST(6);
      if (arrive(&sm.ca, l) == 7u) {
        f32x4 t[8];
#pragma unroll
        for (int i = 0; i < 8; ++i) t[i] = *(const f32x4*)&sm.acc[i][4 * l];
        const f32x4 t0 = ((t[0] + t[1]) + (t[2] + t[3])) + ((t[4] + t[5]) + (t[6] + t[7]));
        const u32x4 r = {nosent(t0[0]), nosent(t0[1]), nosent(t0[2]), nosent(t0[3])};
        __builtin_amdgcn_raw_buffer_store_b128(r, rWS, WS_OX + (u32)(g * 16 + (l >> 2)) * 1024u + j * 64 + (l & 3) * 16, 0, AUX_SC1);
        STAMP(12);
      }
      FENCE();
      if (w == 8) DWLOADS();
    }
  }
  FENCE();
  float y[8];
  if (ow) {
    const int rs = l >> 3;
    u32 h0, h1, h2;
    for (u32 it = 0;; ++it) {
      h0 = lds_ld(&sm.hv[rs]); h1 = lds_ld(&sm.hv[8 + rs]); h2 = lds_ld(&sm.hv[16 + rs]);
      if (__all(h0 != SENT && h1 != SENT && h2 != SENT) || it >= SPIN_L) break;
      __builtin_amdgcn_s_sleep(1);
    }
#pragma unroll
    for (int e = 0; e < 4; ++e) {
      y[2 * e] = __uint_as_float(h0) * bflo(dw[0][e]) + __uint_as_float(h1) * bflo(dw[1][e]) + __uint_as_float(h2) * bflo(dw[2][e]);
      y[2 * e + 1] = __uint_as_float(h0) * bfhi(dw[0][e]) + __uint_as_float(h1) * bfhi(dw[1][e]) + __uint_as_float(h2) * bfhi(dw[2][e]);
    }
    if (w == 0) STAMP(13);
  }
  FENCE();
  if (!ow) {
    const int p0 = w - 9;
    u32 wb_;
    for (u32 it = 0;; ++it) {
      wb_ = lds_ld(&sm.wgt[(p0 + 7 * (l >> 4)) & 15]);
      if (__all(wb_ != SENT) || it >= SPIN_L) break;
      __builtin_amdgcn_s_sleep(1);
    }
    const unsigned char* op = ws + WS_OX + (u32)u * 1024u + (u32)p0 * 64u;
    u32 pend = p0 < 2 ? 7u : 3u;
    for (u32 it = 0; it < SPIN_G && pend; ++it) {
      u32x16 v[3];
      if (pend & 1u) asm volatile("s_load_dwordx16 %0, %1, 0x0 glc" : "=&s"(v[0]) : "s"(op) : "memory");
      if (pend & 2u) asm volatile("s_load_dwordx16 %0, %1, 0x1c0 glc" : "=&s"(v[1]) : "s"(op) : "memory");
      if (pend & 4u) asm volatile("s_load_dwordx16 %0, %1, 0x380 glc" : "=&s"(v[2]) : "s"(op) : "memory");
      asm volatile("s_waitcnt lgkmcnt(0)" ::: "memory");
#pragma unroll
      for (int k = 0; k < 3; ++k) {
        if (pend & (1u << k)) {
          asm volatile("" : "+s"(v[k]));
          u32 mx = v[k][0];
#pragma unroll
          for (int e = 1; e < 16; ++e) mx = mx > v[k][e] ? mx : v[k][e];
          if (mx != SENT) {
            pend &= ~(1u << k);
            u32 t = 0;
#pragma unroll
            for (int e = 0; e < 16; ++e) asm("v_writelane_b32 %0, %1, %2" : "+v"(t) : "s"(v[k][e]), "n"(16 * k + e));
            const u32 r = nosent(__uint_as_float(t) * __uint_as_float(wb_));
            const int p = p0 + 7 * k;
            if ((l >> 4) == k) lds_st(&sm.op[(((l >> 1) & 7) * 8 + (p >> 1)) * 4 + (l & 1) * 2 + (p & 1)], r);
          }
        }
      }
    }
    if (pend) { lds_st(&sm.op[4 * l], 0u); lds_st(&sm.op[4 * l + 1], 0u); lds_st(&sm.op[4 * l + 2], 0u); lds_st(&sm.op[4 * l + 3], 0u); }
    if (w == 9) STAMP(20);
    if (w == 13) STAMP(15);
  }
  if (ow) {
    float wf[16];
#pragma unroll
    for (int e = 0; e < 4; ++e) { wf[2 * e] = bflo(dw[3][e]); wf[2 * e + 1] = bfhi(dw[3][e]); wf[8 + 2 * e] = bflo(dw[4][e]); wf[9 + 2 * e] = bfhi(dw[4][e]); }
    if (w == 0) STAMP(21);
    u32x4 o4;
    for (u32 it = 0;; ++it) {
      o4 = lds_ld4(&sm.op[4 * l]);
      if (__all(o4[0] != SENT && o4[1] != SENT && o4[2] != SENT && o4[3] != SENT) || it >= SPIN_L) break;
    }
    float o0 = __uint_as_float(o4[0]) + __uint_as_float(o4[1]), o1 = __uint_as_float(o4[2]) + __uint_as_float(o4[3]);
    o0 += dpp<0xB1>(o0); o1 += dpp<0xB1>(o1);
    o0 += dpp<0x4E>(o0); o1 += dpp<0x4E>(o1);
    o0 += dpp<0x141>(o0); o1 += dpp<0x141>(o1);
#pragma unroll
    for (int e = 0; e < 8; ++e) y[e] += o0 * wf[e] + o1 * wf[8 + e];
    float a4[4], a2[2];
#pragma unroll
    for (int e = 0; e < 4; ++e) a4[e] = red32(y[e], y[e + 4]);
#pragma unroll
    for (int e = 0; e < 2; ++e) a2[e] = red16(a4[e], a4[e + 2]);
    const float t_ = (l & 8) ? a2[0] : a2[1], kp_ = (l & 8) ? a2[1] : a2[0];
    const float a1 = kp_ + dpp<0x128>(t_);
    __builtin_amdgcn_raw_buffer_store_b32(nosent(a1), rWS, WS_FX + (u32)(8 * w + (l & 7)) * 8192u + (u32)u * 32u +
                                          (4 * (l >> 5) + 2 * ((l >> 4) & 1) + ((l >> 3) & 1)) * 4, 0, AUX_SC1);
    if (w == 0) STAMP(14);
  }
  if (fcons) {
    const int pw = w & 7;
    if (w >= 9) {
      for (u32 it = 0; it < SPIN_L; ++it) {
        const u32x4 o4 = lds_ld4(&sm.op[4 * l]);
        if (__all(o4[0] != SENT && o4[1] != SENT && o4[2] != SENT && o4[3] != SENT)) break;
        __builtin_amdgcn_s_sleep(2);
      }
    }
#define FDELAY 4
    __builtin_amdgcn_s_sleep(FDELAY);
    if (w >= 8) __builtin_amdgcn_s_sleep(5);
    const u32 xo = *((lds_u16*)sm.xs + cidx * 8 + (l & 7));
    const u32 fb = WS_FX + cidx * 8192u + pw * 1024 + l * 16;
    u32x4 t;
    bool have = false;
    for (u32 it = 0; it < SPIN_G; ++it) {
      asm volatile("" ::: "memory");
      t = __builtin_amdgcn_raw_buffer_load_b128(rWS, fb, 0, AUX_SC1);
      if (__all(t[0] != SENT && t[1] != SENT && t[2] != SENT && t[3] != SENT)) { have = true; break; }
      if (lds_ld(&sm.fin[pw * 8]) != SENT) break;
    }
    if (have) {
      const float b0 = red32(__uint_as_float(t[0]), __uint_as_float(t[2])), b1 = red32(__uint_as_float(t[1]), __uint_as_float(t[3]));
      float c = red16(b0, b1);
      c += dpp<0x4E>(c); c += dpp<0x124>(c); c += dpp<0x128>(c);
      if ((l & 14) == 0) lds_st(&sm.fin[pw * 8 + 4 * (l & 1) + 2 * (l >> 5) + ((l >> 4) & 1)], nosent(c));
    }
    if (w == 0) STAMP(22);
    u32 fv;
    for (u32 it = 0;; ++it) {
      fv = lds_ld(&sm.fin[l]);
      if (__all(fv != SENT) || it >= SPIN_L) break;
    }
    float c = __uint_as_float(fv);
    c += dpp<0x128>(c); c = red16(c, c); c = red32(c, c);
    const __bf16 r = (__bf16)(bflo(xo) + c);
    if (l < 8) out[cidx * 8 + l] = __builtin_bit_cast(unsigned short, r);
    if (cidx == 0 && l == 0) __builtin_amdgcn_raw_buffer_store_b32(sm.ev + 1u, rWS, WS_E, 0, AUX_SC1);
    if (w == 0) STAMP(23);
  }
}

extern "C" void kernel_launch(void* const* d_in, const int* in_sizes, int n_in, void* d_out, int out_size, void* d_ws, size_t ws_size,
                              hipStream_t stream) {
  fused_decode<<<256, 1024, 0, stream>>>((const unsigned short*)d_in[0], (const unsigned short*)d_in[1], (const unsigned short*)d_in[2],
                                         (const unsigned short*)d_in[3], (const unsigned short*)d_in[4], (const unsigned short*)d_in[6],
                                         (const unsigned short*)d_in[7], (const unsigned short*)d_in[8], (unsigned char*)d_ws,
                                         (unsigned short*)d_out);
}
```

```cpp
#include <hip/hip_runtime.h>
#include <hip/hip_bf16.h>
#include <cstdint>

typedef unsigned u32;
typedef u32 u32x4 __attribute__((ext_vector_type(4)));
typedef u32 u32x2 __attribute__((ext_vector_type(2)));
typedef u32 u32x16 __attribute__((ext_vector_type(16)));
typedef float f32x4 __attribute__((ext_vector_type(4)));
typedef float f32x2 __attribute__((ext_vector_type(2)));
typedef __bf16 bf16x2 __attribute__((ext_vector_type(2)));
typedef __bf16 bf16x8 __attribute__((ext_vector_type(8)));
typedef __amdgpu_buffer_rsrc_t rsrc_t;

#define WIN 3

constexpr u32 WS_E = 0;
constexpr u32 WS_QX = 4096;
constexpr u32 WS_HX = WS_QX + 256 * 512;
constexpr u32 WS_FL = WS_HX + 256 * 1024;
constexpr u32 WS_AG = WS_FL + 256 * 128;
constexpr u32 WS_ML = WS_AG + 16 * 512;
constexpr u32 WS_OX = WS_ML + 256 * 128;
constexpr u32 WS_FX = WS_OX + 256 * 1024;
constexpr u32 WS_END = WS_FX + 72 * 8192;
constexpr u32 SPIN_G = 40000;
constexpr u32 SPIN_L = 4000000;
constexpr u32 SENT = 0xFFFFFFFFu;
constexpr u32 OOR = 0xFFF00000u;

#define STAMP(i) do { } while (0)
#define WST(id) do { } while (0)

#define AUX_NT 2
#define AUX_SC1 16
#define FENCE() __builtin_amdgcn_sched_barrier(0)
#define THR(n) do { asm volatile("s_waitcnt vmcnt(%0)" :: "n"(n) : "memory"); FENCE(); } while (0)
#define THROTTLE() THR(WIN - 1)
#define LDSWAIT() asm volatile("s_waitcnt lgkmcnt(0)" ::: "memory")

__device__ __forceinline__ float bflo(u32 u) { return __uint_as_float(u << 16); }
__device__ __forceinline__ float bfhi(u32 u) { return __uint_as_float(u & 0xffff0000u); }
template <int CTRL> __device__ __forceinline__ float dpp(float x) {
  return __int_as_float(__builtin_amdgcn_update_dpp(0, __float_as_int(x), CTRL, 0xf, 0xf, true));
}
template <int N> __device__ __forceinline__ float rorf(float x) {
  if constexpr ((N & 15) == 0) return x; else return dpp<0x120 + (N & 15)>(x);
}
template <int N> __device__ __forceinline__ u32 roru(u32 x) {
  if constexpr ((N & 15) == 0) return x; else return (u32)__builtin_amdgcn_update_dpp(0, (int)x, 0x120 + (N & 15), 0xf, 0xf, true);
}
__device__ __forceinline__ float red32(float a, float b) {
  auto r = __builtin_amdgcn_permlane32_swap(__float_as_uint(a), __float_as_uint(b), false, false);
  return __uint_as_float(r[0]) + __uint_as_float(r[1]);
}
__device__ __forceinline__ float red16(float a, float b) {
  auto r = __builtin_amdgcn_permlane16_swap(__float_as_uint(a), __float_as_uint(b), false, false);
  return __uint_as_float(r[0]) + __uint_as_float(r[1]);
}
__device__ __forceinline__ float max32(float a) {
  auto r = __builtin_amdgcn_permlane32_swap(__float_as_uint(a), __float_as_uint(a), false, false);
  return fmaxf(__uint_as_float(r[0]), __uint_as_float(r[1]));
}
__device__ __forceinline__ float max16(float a) {
  auto r = __builtin_amdgcn_permlane16_swap(__float_as_uint(a), __float_as_uint(a), false, false);
  return fmaxf(__uint_as_float(r[0]), __uint_as_float(r[1]));
}
__device__ __forceinline__ float rowsum16(float s) {
  s += dpp<0xB1>(s); s += dpp<0x4E>(s); s += dpp<0x141>(s); s += dpp<0x140>(s);
  return s;
}
__device__ __forceinline__ float rowmax16(float s) {
  s = fmaxf(s, dpp<0xB1>(s)); s = fmaxf(s, dpp<0x4E>(s)); s = fmaxf(s, dpp<0x141>(s)); s = fmaxf(s, dpp<0x140>(s));
  return s;
}
__device__ __forceinline__ float wavesum(float s) { s = rowsum16(s); s = red16(s, s); s = red32(s, s); return s; }
__device__ __forceinline__ rsrc_t mkr(const void* p, u32 n) { return __builtin_amdgcn_make_buffer_rsrc((void*)p, 0, n, 0x00020000); }
__device__ __forceinline__ u32x4 gran(float a, float b, u32 tag) { u32x4 r = {__float_as_uint(a), tag, __float_as_uint(b), tag}; return r; }
__device__ __forceinline__ bool tagok(u32x4 v, u32 tag) { return v[1] == tag && v[3] == tag; }
typedef __attribute__((address_space(3))) volatile u32 lds_u32;
typedef __attribute__((address_space(3))) volatile u32x4 lds_u32x4;
typedef __attribute__((address_space(3))) volatile unsigned short lds_u16;
__device__ __forceinline__ u32 lds_ld(u32* p) { return *(lds_u32*)p; }
__device__ __forceinline__ u32x4 lds_ld4(u32* p) { return *(lds_u32x4*)p; }
__device__ __forceinline__ void lds_st(u32* p, u32 v) { *(lds_u32*)p = v; }
#define PIN8(a) asm volatile("" : "+v"(a[0]), "+v"(a[1]), "+v"(a[2]), "+v"(a[3]), "+v"(a[4]), "+v"(a[5]), "+v"(a[6]), "+v"(a[7]))
__device__ __forceinline__ u32 nosent(float v) { const u32 t = __float_as_uint(v); return t == SENT ? 0x7FC00000u : t; }
__device__ __forceinline__ u32 arrive(u32* c, int l) {
  u32 old = 0;
  LDSWAIT();
  if (l == 0) old = __hip_atomic_fetch_add(c, 1u, __ATOMIC_RELAXED, __HIP_MEMORY_SCOPE_WORKGROUP);
  old = __builtin_amdgcn_readfirstlane(old);
  asm volatile("" ::: "memory");
  return old;
}
__device__ __forceinline__ f32x4 mfma(u32x4 a, u32x4 b, f32x4 c) {
  return __builtin_amdgcn_mfma_f32_16x16x32_bf16(__builtin_bit_cast(bf16x8, a), __builtin_bit_cast(bf16x8, b), c, 0, 0, 0);
}

struct __align__(16) Smem {
  float qp[8][64];
  float hs[12][64];
  float acc[8][256];
  float wmax[2][8];
  float wl[2][8];
  u32 qv[2][2][64];
  u32 xs[320];
  float xn[640];
  u32 op[256];
  u32 fin[64];
  u32 hv[24];
  u32 wgt[16];
  u32 cq, ch, cm, ca, fq, fx, cv, ff;
  u32 ev, pad3, pad4, pad5;
};
#define WA 3
#define NA 3
#define WB 6
#define NB 32
#define WC 6
#define NC 32
#define WD 6
#define BARI 2
#define TRIGN 8u
#define QSLP 4
#define HSLP 8
#define HSLP0 32
#define AUX_1ST 0
#define MSLP 4
#define MSLP0 16
#define AUX_E 0
#define WS_ 3
#define QPOS 7
#define TRIG 15
#define PACE_AT(base, T) do {                                                                                               \
      asm volatile("1: s_memrealtime s[94:95]\n s_waitcnt lgkmcnt(0)\n s_sub_u32 s94, s94, %0\n s_cmpk_lt_i32 s94, %1\n"    \
                   " s_cbranch_scc0 2f\n s_sleep 1\n s_branch 1b\n2:"                                                      \
                   :: "s"(base), "n"(T) : "s94", "s95", "scc", "memory");                                                   \
      FENCE(); } while (0)
#define PN0 3
#define PT0 2150
#define N1 4
#define N2 17
#define TAU1 285
#define TAU2 380
#define TAU3 285
constexpr int TK(int n) { return n <= N1 ? PT0 + (n - PN0) * TAU1 : n <= N2 ? TK(N1) + (n - N1) * TAU2 : TK(N2) + (n - N2) * TAU3; }
#define GT0 2600
#define GTAU 800
#define DOFF 500
#define DTAU 280
#define DSTAG 0
#define BUMP(c) do { if (l == 0) __hip_atomic_fetch_add(&sm.c, 1u, __ATOMIC_RELAXED, __HIP_MEMORY_SCOPE_WORKGROUP); } while (0)
#define LWAIT(c, n) do { for (u32 it = 0; lds_ld(&sm.c) != (n) && it < SPIN_L; ++it) __builtin_amdgcn_s_sleep(1); asm volatile("" ::: "memory"); } while (0)

__global__ void __launch_bounds__(1024, 4)
fused_decode(const unsigned short* __restrict__ x, const unsigned short* __restrict__ Kc, const unsigned short* __restrict__ Vc,
             const unsigned short* __restrict__ wq, const unsigned short* __restrict__ wo, const unsigned short* __restrict__ wg,
             const unsigned short* __restrict__ wu, const unsigned short* __restrict__ wd, unsigned char* __restrict__ ws,
             unsigned short* __restrict__ out) {
  __shared__ Smem sm;
  const int b = blockIdx.x;
  const int w = __builtin_amdgcn_readfirstlane(threadIdx.x >> 6);
  const int l = threadIdx.x & 63;
#define PXA 0
#define PXB 0
  const int u = ((b + PXA) & 7) * 32 + ((b >> 3) ^ PXB);
  const int g = u >> 4, j = u & 15, k4 = u & 3;
  const bool fcons = (u & 31) < 9;
  const u32 cidx = (u32)((u >> 5) * 9 + (u & 31));
  const u32 tb = (u32)__builtin_amdgcn_s_memrealtime();
  FENCE();
  asm volatile("" :: "s"(x), "s"(Kc), "s"(Vc), "s"(wq), "s"(wo), "s"(wg), "s"(wu), "s"(wd), "s"(ws));
  const rsrc_t rWS = mkr(ws, WS_END);
  const bool ow = w < 9;
  const rsrc_t rD = mkr(wd, 4608u * 1152u), rO = mkr(wo, 4096u * 1152u);
  const u32 co = (u32)w * 128u + (l & 7) * 16;
  const u32 od = ow ? (u32)(18 * u + (l >> 3)) * 1152u + co : OOR;
  const u32 oo = ow ? (u32)(16 * u + (l >> 3)) * 1152u + co : OOR;
  u32x4 dw[5];
#define DWLOADS() do {                                                                                     \
    dw[0] = __builtin_amdgcn_raw_buffer_load_b128(rD, od, 0, AUX_NT);                                      \
    dw[1] = __builtin_amdgcn_raw_buffer_load_b128(rD, od, 8 * 1152, AUX_NT);                               \
    dw[2] = __builtin_amdgcn_raw_buffer_load_b128(rD, l < 16 ? od : OOR, 16 * 1152, AUX_NT);               \
    dw[3] = __builtin_amdgcn_raw_buffer_load_b128(rO, oo, 0, AUX_NT);                                      \
    dw[4] = __builtin_amdgcn_raw_buffer_load_b128(rO, oo, 8 * 1152, AUX_NT); FENCE(); } while (0)

  if (w < 8) {
    const u32 ts0 = tb + (u32)(w * (GTAU / 60)), td0 = tb + (u32)(w * (DSTAG / 10));
    const rsrc_t rX = mkr(x, 1152), rQ = mkr(wq, 576u * 8192u), rG = mkr((g & 1) ? wu : wg, 576u * 9216u);
    const int rb = 144 * k4 + 18 * w;
    const u32 qo = (u32)(rb + (l >> 3)) * 8192u + (u32)(256 * g + 64 * (j >> 2)) * 2 + (l & 7) * 16;
    const int uB = 9 * j + 3 * (w >> 1) + 1, uA = uB - 1 + 2 * (w & 1);
    const int rA = 36 * (uA & 15), rB = 36 * (uB & 15) + 18 * (w & 1);
    const u32 gv = w < 6 ? (u32)(l >> 3) * 9216u + (u32)(u >> 5) * 1152u + (l & 7) * 16 : OOR;
    const u32 gB = w < 6 ? (u32)(rB * 9216 + (uB >> 4) * 128) : 0u, gA = (u32)(rA * 9216 + (uA >> 4) * 128);
#define AUX_G AUX_NT
    u32x4 xa = {0u, 0u, 0u, 0u};
    u32 xb = 0u, Ev = 0u;
    if (w == 0) {
      xa = __builtin_amdgcn_raw_buffer_load_b128(rX, l * 16, 0, 0);
      xb = __builtin_amdgcn_raw_buffer_load_b32(rX, 1024 + l * 4, 0, 0);
      Ev = __builtin_amdgcn_raw_buffer_load_b32(rWS, WS_E, 0, AUX_E);
    }
    {
      const u32x4 sv = {SENT, SENT, SENT, SENT};
      if (w == 7) __builtin_amdgcn_raw_buffer_store_b128(sv, rWS, WS_OX + (u32)u * 1024u + l * 16, 0, AUX_SC1);
      if (w == 6) __builtin_amdgcn_raw_buffer_store_b128(sv, rWS, l < 8 ? WS_ML + (u32)u * 128u + l * 16 : OOR, 0, AUX_SC1);
      if (fcons) __builtin_amdgcn_raw_buffer_store_b128(sv, rWS, WS_FX + cidx * 8192u + (u32)w * 1024u + l * 16, 0, AUX_SC1);
    }
    FENCE();
    u32x4 fq_[3], fg[7];
    fq_[0] = __builtin_amdgcn_raw_buffer_load_b128(rQ, qo, 0, AUX_NT); FENCE();
    fq_[1] = __builtin_amdgcn_raw_buffer_load_b128(rQ, qo, 8 * 8192, AUX_NT); FENCE();
    fq_[2] = __builtin_amdgcn_raw_buffer_load_b128(rQ, l < 16 ? qo : OOR, 16 * 8192, AUX_NT); FENCE();
    if (threadIdx.x < 368) sm.op[threadIdx.x] = threadIdx.x < 360 ? SENT : 0u;
    LDSWAIT(); __builtin_amdgcn_s_barrier(); FENCE(); WST(0);
    fg[0] = __builtin_amdgcn_raw_buffer_load_b128(rG, gv, gB, AUX_G); FENCE();
    if (w == 0) {
      float xf[10];
#pragma unroll
      for (int e = 0; e < 4; ++e) { xf[2 * e] = bflo(xa[e]); xf[2 * e + 1] = bfhi(xa[e]); }
      xf[8] = bflo(xb); xf[9] = bfhi(xb);
      const float mean = wavesum((((xf[0] + xf[1]) + (xf[2] + xf[3])) + ((xf[4] + xf[5]) + (xf[6] + xf[7]))) + (xf[8] + xf[9])) * (1.0f / 576.0f);
#pragma unroll
      for (int e = 0; e < 10; ++e) xf[e] -= mean;
      float v0 = xf[0] * xf[0], v1 = xf[1] * xf[1];
#pragma unroll
      for (int e = 2; e < 8; e += 2) { v0 += xf[e] * xf[e]; v1 += xf[e + 1] * xf[e + 1]; }
      v0 += l < 32 ? xf[8] * xf[8] + xf[9] * xf[9] : 0.f;
      const float rstd = __builtin_amdgcn_rsqf(wavesum(v0 + v1) * (1.0f / 576.0f) + 1e-5f);
      *(f32x4*)&sm.xn[8 * l] = f32x4{xf[0] * rstd, xf[1] * rstd, xf[2] * rstd, xf[3] * rstd};
      *(f32x4*)&sm.xn[8 * l + 4] = f32x4{xf[4] * rstd, xf[5] * rstd, xf[6] * rstd, xf[7] * rstd};
      *(f32x2*)&sm.xn[512 + 2 * l] = f32x2{xf[8] * rstd, xf[9] * rstd};
      *(u32x4*)&sm.xs[4 * l] = xa;
      sm.xs[256 + l] = xb;
      if (l == 0) sm.ev = Ev;
      LDSWAIT();
      if (l == 0) lds_st(&sm.fx, 1u);
      STAMP(2);
    }
    FENCE();
    LWAIT(fx, 1u);
    const u32 tag = __builtin_amdgcn_readfirstlane(sm.ev) * 0x9E3779B1u + 0x7F4A7C15u;
    float xq[3], xg[8];
#pragma unroll
    for (int i = 0; i < 3; ++i) xq[i] = sm.xn[rb + (l >> 3) + 8 * i];
    asm volatile("" : "+v"(xq[0]), "+v"(xq[1]), "+v"(xq[2]));
    FENCE();
    THR(1);
    WST(1);
#define FACC(N, G, F, X) do {                                                                                          \
      _Pragma("unroll") for (int e = 0; e < 4; ++e) G[e] = f32x2{X[0], X[0]} * f32x2{bflo(F[0][e]), bfhi(F[0][e])};    \
      _Pragma("unroll") for (int i = 1; i < N; ++i)                                                                    \
        _Pragma("unroll") for (int e = 0; e < 4; ++e) G[e] += f32x2{X[i], X[i]} * f32x2{bflo(F[i][e]), bfhi(F[i][e])}; \
    } while (0)
    {
      f32x2 g2[4];
      FACC(3, g2, fq_, xq);
      float a4[4], a2[2];
#pragma unroll
      for (int e = 0; e < 4; ++e) a4[e] = red32(g2[e >> 1][e & 1], g2[2 + (e >> 1)][e & 1]);
#pragma unroll
      for (int e = 0; e < 2; ++e) a2[e] = red16(a4[e], a4[e + 2]);
      const float t_ = (l & 8) ? a2[0] : a2[1], kp_ = (l & 8) ? a2[1] : a2[0];
      sm.qp[w][8 * (l & 7) + 4 * (l >> 5) + 2 * ((l >> 4) & 1) + ((l >> 3) & 1)] = kp_ + dpp<0x128>(t_);
      if (w == 0) STAMP(1);
      if (arrive(&sm.cq, l) == 7u) {
        const int P_ = l & 31, hf = l >> 5;
        const f32x2 s_ = (*(const f32x2*)&sm.qp[hf * 4][2 * P_] + *(const f32x2*)&sm.qp[hf * 4 + 1][2 * P_]) +
                         (*(const f32x2*)&sm.qp[hf * 4 + 2][2 * P_] + *(const f32x2*)&sm.qp[hf * 4 + 3][2 * P_]);
        const float s0 = red32(s_[0], s_[0]), s1 = red32(s_[1], s_[1]);
        if (l < 32) __builtin_amdgcn_raw_buffer_store_b128(gran(s0, s1, tag), rWS, WS_QX + (u32)u * 512u + P_ * 16, 0, AUX_SC1);
        const u32 fa = l < 16 ? WS_FL + (u32)(g * 16 + l) * 128u + j * 4 : (fcons && l < 32) ? WS_AG + (u32)(l - 16) * 512u + cidx * 4u : OOR;
        __builtin_amdgcn_raw_buffer_store_b32(tag, rWS, fa, 0, AUX_SC1);
        STAMP(3);
      }
      FENCE();
    }
    if (w < 6) {
      const float* const xr = &sm.xn[l >> 3];
      xg[0] = xr[rB]; xg[1] = xr[rB + 8];
      xg[2] = xr[l < 16 ? rB + 16 : rA - 2];
      xg[4] = xr[rA + 6]; xg[5] = xr[rA + 14]; xg[6] = xr[rA + 22]; xg[7] = xr[rA + 30];
      xg[7] = l < 48 ? xg[7] : 0.f;
      xg[3] = l < 16 ? 0.f : xg[2];
      xg[2] = l < 16 ? xg[2] : 0.f;
      const int c0 = (9 * j) >> 4, n0 = 16 - ((9 * j) & 15);
      const int s0 = j - ((c0 * 455) >> 8);
      const int c1 = n0 < 9 ? c0 + 1 : j == 0 ? 0 : j == 6 ? 4 : 8, s1 = n0 < 9 ? 0 : 2;
      const u32 cA = (u32)(64 * c0 + l), cB = (u32)(64 * c1 + l), qA = (cA * 3641u) >> 16, qB = (cB * 3641u) >> 16;
      const u32 hbase = WS_HX + (u32)(u & ~31) * 1024u + (u32)(g & 1) * 432u;
      u32 hA = hbase + (u32)s0 * 144u + qA * 1024u + (cA - 18u * qA) * 8u;
      u32 hB = (n0 < 9 || j == 0 || j == 6 || j == 15) ? hbase + (u32)s1 * 144u + qB * 1024u + (cB - 18u * qB) * 8u : OOR;
      u32 gv2 = gv + (l < 16 ? gB + 16u * 9216u : gA - 2u * 9216u), gv6 = l < 48 ? gv : OOR;
      PIN8(xg);
      asm volatile("" : "+v"(hA), "+v"(hB), "+v"(gv2), "+v"(gv6));
      const int cl = 8 * (l & 7) + 4 * (l >> 5) + 2 * ((l >> 4) & 1) + ((l >> 3) & 1);
#define GLD(k, vo, so) do { PACE_AT(ts0, (GT0 + ((k) - 1) * GTAU) / 10); fg[k] = __builtin_amdgcn_raw_buffer_load_b128(rG, vo, so, AUX_G); FENCE(); } while (0)
#define COLSUM(G) ({ float a4_[4], a2_[2];                                                                              \
      _Pragma("unroll") for (int e = 0; e < 4; ++e) a4_[e] = red32(G[e >> 1][e & 1], G[2 + (e >> 1)][e & 1]);           \
      _Pragma("unroll") for (int e = 0; e < 2; ++e) a2_[e] = red16(a4_[e], a4_[e + 2]);                                 \
      const float t_ = (l & 8) ? a2_[0] : a2_[1], kp_ = (l & 8) ? a2_[1] : a2_[0];                                      \
      kp_ + dpp<0x128>(t_); })
      GLD(1, gv, gB + 8u * 9216u); GLD(2, gv2, 0); GLD(3, gv, gA + 6u * 9216u); GLD(4, gv, gA + 14u * 9216u);
      {
        f32x2 g2[4];
        FACC(3, g2, fg, xg);
        sm.hs[2 * (w & 6) + 1 + (w & 1)][cl] = COLSUM(g2);
        FENCE();
      }
      GLD(5, gv, gA + 22u * 9216u); GLD(6, gv6, gA + 30u * 9216u);
      {
        f32x2 g2[4];
        FACC(5, g2, (fg + 2), (xg + 3));
        sm.hs[2 * (w & 6) + 3 * (w & 1)][cl] = COLSUM(g2);
        WST(2);
        if (arrive(&sm.ch, l) == 5u) {
          float t[12], a0 = 0.f, a1 = 0.f;
#pragma unroll
          for (int i = 0; i < 12; ++i) t[i] = sm.hs[i][l];
#pragma unroll
          for (int i = 0; i < 12; ++i) {
            const bool first = 3 * (i >> 2) + ((i & 3) + 1) / 2 < n0;
            a0 += first ? t[i] : 0.f; a1 += first ? 0.f : t[i];
          }
          const u32x2 r0 = {__float_as_uint(a0), tag}, r1 = {__float_as_uint(a1), tag};
          __builtin_amdgcn_raw_buffer_store_b64(r0, rWS, hA, 0, AUX_SC1);
          __builtin_amdgcn_raw_buffer_store_b64(r1, rWS, hB, 0, AUX_SC1);
          STAMP(6);
        }
        FENCE();
      }
    } else {
      if (w == 6) {
        const unsigned char* fp = ws + WS_FL + (u32)u * 128u;
        LWAIT(cq, 8u);
        for (u32 it = 0; it < SPIN_G; ++it) {
          u32x16 v;
          __builtin_amdgcn_s_sleep(QSLP);
          asm volatile("s_load_dwordx16 %0, %1, 0x0 glc\n s_waitcnt lgkmcnt(0)" : "=&s"(v) : "s"(fp) : "memory");
          u32 bad = 0;
#pragma unroll
          for (int e = 0; e < 16; ++e) bad |= v[e] ^ tag;
          if (bad == 0) break;
        }
        if (l == 0) lds_st(&sm.ff, 1u);
      } else {
        LWAIT(ff, 1u);
      }
      if (w == 6) STAMP(4);
      const u32 g1a = WS_QX + (u32)(g * 16 + (w - 6) * 8 + (l >> 4)) * 512u + (l & 15) * 16;
      u32x4 g1[4];
#pragma unroll
      for (int i = 0; i < 4; ++i) g1[i] = __builtin_amdgcn_raw_buffer_load_b128(rWS, g1a + (i & 1) * 256 + (i >> 1) * 2048, 0, AUX_1ST);
      asm volatile("" : "+v"(g1[0]), "+v"(g1[1]), "+v"(g1[2]), "+v"(g1[3]));
      if (w == 6) STAMP(16);
      if (!__all(tagok(g1[0], tag) && tagok(g1[1], tag) && tagok(g1[2], tag) && tagok(g1[3], tag))) {
        for (u32 it = 0;; ++it) {
          asm volatile("" ::: "memory");
#pragma unroll
          for (int i = 0; i < 4; ++i) g1[i] = __builtin_amdgcn_raw_buffer_load_b128(rWS, g1a + (i & 1) * 256 + (i >> 1) * 2048, 0, AUX_SC1);
          asm volatile("" : "+v"(g1[0]), "+v"(g1[1]), "+v"(g1[2]), "+v"(g1[3]));
          if (__all(tagok(g1[0], tag) && tagok(g1[1], tag) && tagok(g1[2], tag) && tagok(g1[3], tag)) || it >= SPIN_G) break;
        }
      }
      const float qs = 0.08838834764831845f * 1.4426950408889634f;
#pragma unroll
      for (int i = 0; i < 4; ++i) {
        float a = red16(__uint_as_float(g1[i][0]), __uint_as_float(g1[i][2]));
        a = red32(a, a) * qs;
        const u32 hib = __builtin_bit_cast(unsigned short, (__bf16)a);
        const u32 lob = __builtin_bit_cast(unsigned short, (__bf16)(a - bflo(hib)));
        *((lds_u16*)&sm.qv[w - 6][l >> 5][16 * i + (l & 15)] + ((l >> 4) & 1)) = (unsigned short)(l < 32 ? hib : lob);
      }
      LDSWAIT();
      BUMP(fq);
      if (w == 6) STAMP(5);
      WST(3);
    }
    FENCE();
    if (w == 7) {
      const bool on = (l & 15) < 9 && l < 48;
      const u32 hb = on ? WS_HX + (u32)u * 1024u + (u32)(l >> 4) * 144u + (l & 15) * 16 : OOR;
      const u32 ab = l < 18 ? WS_AG + (u32)g * 512u + l * 16 : OOR;
      const unsigned char* hs = ws + WS_HX + (u32)u * 1024u;
      LWAIT(ch, 6u);
      for (u32 it = 0; it < SPIN_G; ++it) {
        u32 a0, a6; u32x4 a1, a2, a3, a4, a5;
        __builtin_amdgcn_s_sleep(HSLP);
        asm volatile("s_load_dword %0, %7, 0x4 glc\n s_load_dwordx4 %1, %7, 0x8c glc\n s_load_dwordx4 %2, %7, 0x11c glc\n s_load_dwordx4 %3, %7, 0x1ac glc\n"
                     " s_load_dwordx4 %4, %7, 0x23c glc\n s_load_dwordx4 %5, %7, 0x2cc glc\n s_load_dword %6, %7, 0x35c glc\n s_waitcnt lgkmcnt(0)"
                     : "=&s"(a0), "=&s"(a1), "=&s"(a2), "=&s"(a3), "=&s"(a4), "=&s"(a5), "=&s"(a6) : "s"(hs) : "memory");
        if (((a0 ^ tag) | (a6 ^ tag) | (a1[0] ^ tag) | (a1[2] ^ tag) | (a2[0] ^ tag) | (a2[2] ^ tag) | (a3[0] ^ tag) | (a3[2] ^ tag) |
             (a4[0] ^ tag) | (a4[2] ^ tag) | (a5[0] ^ tag) | (a5[2] ^ tag)) == 0) break;
      }
      u32x4 t0, t1;
      for (u32 it = 0;; ++it) {
        asm volatile("" ::: "memory");
        const u32x4 e0 = __builtin_amdgcn_raw_buffer_load_b128(rWS, ab, 0, AUX_SC1);
        t0 = __builtin_amdgcn_raw_buffer_load_b128(rWS, hb, 0, AUX_SC1);
        t1 = __builtin_amdgcn_raw_buffer_load_b128(rWS, hb + 432, 0, AUX_SC1);
        if (__all((l >= 18 || (tagok(e0, tag) && e0[0] == tag && e0[2] == tag)) && (!on || (tagok(t0, tag) && tagok(t1, tag)))) || it >= SPIN_G) break;
        __builtin_amdgcn_s_sleep(8);
      }
      float gs = red16(__uint_as_float(t0[0]), __uint_as_float(t0[2])), us = red16(__uint_as_float(t1[0]), __uint_as_float(t1[2]));
      gs = red32(gs, gs); us = red32(us, us);
      const float hval = on ? gs / (1.0f + __expf(-gs)) * us : 0.f;
      if (l < 32 && (l & 15) < 12) lds_st(&sm.hv[2 * (l & 15) + (l >> 4)], nosent(hval));
      STAMP(10);
    } else if (w == 6) {
      LWAIT(cm, 8u);
      const int h = (l >> 3) & 1;
      const float mi = sm.wmax[h][l & 7], li = sm.wl[h][l & 7];
      float M = fmaxf(mi, dpp<0xB1>(mi)); M = fmaxf(M, dpp<0x4E>(M)); M = fmaxf(M, dpp<0x141>(M));
      float L = li * __builtin_amdgcn_exp2f(mi - M);
      L += dpp<0xB1>(L); L += dpp<0x4E>(L); L += dpp<0x141>(L);
      const u32x2 ml = {nosent(M), nosent(L)};
      __builtin_amdgcn_raw_buffer_store_b64(ml, rWS, l < 16 ? WS_ML + (u32)(g * 16 + l) * 128u + j * 8 : OOR, 0, AUX_SC1);
      STAMP(17);
      __builtin_amdgcn_s_sleep(MSLP0);
      const unsigned char* mp = ws + WS_ML + (u32)u * 128u;
      u32x16 v[2];
      for (u32 it = 0; it < SPIN_G; ++it) {
        asm volatile("s_load_dwordx16 %0, %1, 0x0 glc" : "=&s"(v[0]) : "s"(mp) : "memory");
        asm volatile("s_load_dwordx16 %0, %1, 0x40 glc" : "=&s"(v[1]) : "s"(mp) : "memory");
        asm volatile("s_waitcnt lgkmcnt(0)" ::: "memory");
        u32 mx = 0;
#pragma unroll
        for (int k = 0; k < 2; ++k) {
          asm volatile("" : "+s"(v[k]));
#pragma unroll
          for (int e = 0; e < 16; ++e) mx = mx > v[k][e] ? mx : v[k][e];
        }
        if (mx != SENT) break;
        __builtin_amdgcn_s_sleep(MSLP);
      }
      u32 tm = 0, tl = 0;
#pragma unroll
      for (int k = 0; k < 2; ++k)
#pragma unroll
        for (int e = 0; e < 8; ++e) {
          asm("v_writelane_b32 %0, %1, %2" : "+v"(tm) : "s"(v[k][2 * e]), "n"(8 * k + e));
          asm("v_writelane_b32 %0, %1, %2" : "+v"(tl) : "s"(v[k][2 * e + 1]), "n"(8 * k + e));
        }
      asm volatile("s_nop 1" : "+v"(tm), "+v"(tl));
      const float mi2 = __uint_as_float(tm);
      const float e = __builtin_amdgcn_exp2f(mi2 - rowmax16(mi2));
      const float L2 = rowsum16(e * __uint_as_float(tl));
      if (l < 16) lds_st(&sm.wgt[l], nosent(e / L2));
      STAMP(18);
    }
    FENCE();
    for (u32 it = 0; lds_ld(&sm.cv) < TRIGN && it < SPIN_L; ++it) __builtin_amdgcn_s_sleep(1);
    asm volatile("" ::: "memory");
    if (w == 0) STAMP(24);
#define DPACE(k) PACE_AT(td0, (TK(31) + DOFF + (k) * DTAU) / 10)
    DPACE(0); dw[0] = __builtin_amdgcn_raw_buffer_load_b128(rD, od, 0, AUX_NT); FENCE();
    DPACE(1); dw[1] = __builtin_amdgcn_raw_buffer_load_b128(rD, od, 8 * 1152, AUX_NT); FENCE();
    DPACE(2); dw[3] = __builtin_amdgcn_raw_buffer_load_b128(rO, oo, 0, AUX_NT); FENCE();
    DPACE(3); dw[4] = __builtin_amdgcn_raw_buffer_load_b128(rO, oo, 8 * 1152, AUX_NT); FENCE();
    dw[2] = __builtin_amdgcn_raw_buffer_load_b128(rD, l < 16 ? od : OOR, 16 * 1152, AUX_NT); FENCE();
    if (w == 0) STAMP(7);
    FENCE();
  } else {
    const rsrc_t rK = mkr(Kc, 33554432u), rV = mkr(Vc, 33554432u);
#define PKV 0
#define KVI 0
    const u32 kvb = (u32)(u ^ PKV) * 131072u + (u32)(w - 8) * 16384u;
#define TVO(i) (((i) & 3) * 1024)
#define TSO(i) (((i) >> 2) * 4096)
    const u32 voff = kvb + (u32)l * 16u;
    const u32 koff = kvb + (u32)(((l >> 2) & 3) * 256 + (l >> 4) * 64 + (l & 3) * 16);
    u32x4 kk[16], vv[16];
#define WINOF(n) ((n) < NA ? WA : (n) < NB ? WB : (n) < NC ? WC : WD)
#define PACE(n) do { if ((n) >= PN0) PACE_AT(tb, TK(n) / 10); } while (0)
#define KLD(i) do { if ((i) >= WA) THR(WINOF(i) - 1);                                                                      \
      PACE(i);                                                                                                              \
      kk[i] = __builtin_amdgcn_raw_buffer_load_b128(rK, koff + TVO(i), TSO(i), AUX_NT); FENCE();                            \
      if ((i) == BARI) { __builtin_amdgcn_s_barrier(); FENCE(); WST(0); } } while (0)
#define VLD(i) do { THR(WINOF(16 + (i)) - 1);                                                                               \
      PACE(16 + (i));                                                                                                       \
      vv[i] = __builtin_amdgcn_raw_buffer_load_b128(rV, voff + TVO(i), TSO(i), AUX_NT); FENCE();                            \
      if ((i) == TRIG) { BUMP(cv); FENCE(); } } while (0)
    KLD(0); KLD(1); KLD(2); KLD(3); KLD(4); KLD(5); KLD(6); KLD(7);
    WST(1);
    KLD(8); KLD(9); KLD(10); KLD(11); KLD(12); KLD(13); KLD(14); KLD(15);
    WST(2);
    VLD(0); VLD(1); VLD(2); VLD(3); VLD(4); VLD(5);
    if (QPOS >= 6) VLD(6);
    if (QPOS >= 7) VLD(7);
    WST(3);
    LWAIT(fq, 2u);
    if (w == 8) STAMP(8);
    WST(4);
    const int bank = (l >> 2) & 3;
    u32 pk[4];
    float mw;
    {
      const u32x4 qb = *(const u32x4*)&sm.qv[(l >> 1) & 1][l & 1][4 * ((l >> 4) * 4 + bank)];
      float sc[4];
#pragma unroll
      for (int ab = 0; ab < 4; ++ab) {
        const f32x4 z = {0.f, 0.f, 0.f, 0.f};
        const f32x4 d0 = mfma(kk[4 * ab], qb, z), d1 = mfma(kk[4 * ab + 1], qb, z), d2 = mfma(kk[4 * ab + 2], qb, z), d3 = mfma(kk[4 * ab + 3], qb, z);
        float a0 = ((d0[0] + rorf<-4>(d0[1])) + rorf<-8>(d0[2])) + rorf<-12>(d0[3]);
        float a1 = ((d1[1] + rorf<4>(d1[0])) + rorf<-4>(d1[2])) + rorf<-8>(d1[3]);
        float a2 = ((d2[2] + rorf<8>(d2[0])) + rorf<4>(d2[1])) + rorf<-4>(d2[3]);
        float a3 = ((d3[3] + rorf<12>(d3[0])) + rorf<8>(d3[1])) + rorf<4>(d3[2]);
        asm volatile("" : "+v"(a0), "+v"(a1), "+v"(a2), "+v"(a3));
        const float t01 = (l & 4) ? a1 : a0, t23 = (l & 4) ? a3 : a2;
        const float t = (l & 8) ? t23 : t01;
        sc[ab] = t + dpp<0xB1>(t);
        asm volatile("" : "+v"(sc[ab]));
        if (ab == 1) { FENCE(); VLD(QPOS + 1); }
      }
      FENCE(); VLD(QPOS + 2);
      float m = fmaxf(fmaxf(sc[0], sc[1]), fmaxf(sc[2], sc[3]));
      m = fmaxf(m, rorf<4>(m)); m = fmaxf(m, rorf<8>(m));
      m = max32(max16(m));
      mw = m;
#pragma unroll
      for (int ab = 0; ab < 4; ++ab) {
        const float p = __builtin_amdgcn_exp2f(sc[ab] - m);
        const bf16x2 t = {(__bf16)p, (__bf16)p};
        pk[ab] = __builtin_bit_cast(u32, t);
      }
      float lt = (bfhi(pk[0]) + bfhi(pk[1])) + (bfhi(pk[2]) + bfhi(pk[3]));
      lt += rorf<4>(lt); lt += rorf<8>(lt);
      lt = red16(lt, lt); lt = red32(lt, lt);
      if ((l & 61) == 0) { sm.wmax[l >> 1][w - 8] = mw; sm.wl[l >> 1][w - 8] = lt; }
      LDSWAIT();
      BUMP(cm);
    }
    if (w == 8) STAMP(9);
    WST(5);
    FENCE();
    const u32 hm = (l & 1) ? 0xffff0000u : 0x0000ffffu;
    const u32 m0 = bank == 0 ? hm : 0u, m1 = bank == 1 ? hm : 0u, m2 = bank == 2 ? hm : 0u, m3 = bank == 3 ? hm : 0u;
    u32x4 pb[16];
#define PBSET(t) do { pb[t][0] = roru<4 * (0 - ((t) & 3))>(pk[(t) >> 2]) & m0; pb[t][1] = roru<4 * (1 - ((t) & 3))>(pk[(t) >> 2]) & m1;   \
                      pb[t][2] = roru<4 * (2 - ((t) & 3))>(pk[(t) >> 2]) & m2; pb[t][3] = roru<4 * (3 - ((t) & 3))>(pk[(t) >> 2]) & m3;   \
                      asm volatile("" : "+v"(pb[t])); } while (0)
    f32x4 acc = {0.f, 0.f, 0.f, 0.f};
#define PV(i) do { acc = mfma(vv[i], pb[i], acc); asm volatile("" : "+v"(acc)); FENCE(); } while (0)
    if (QPOS < 6) VLD(8);
    if (QPOS < 7) VLD(9);
    PBSET(0); PBSET(1); PBSET(2); PBSET(3); PV(0); PV(1); PV(2); PV(3);
    VLD(10);
    PBSET(4); PBSET(5); PBSET(6); PBSET(7); PV(4);
    VLD(11);
    PV(5); PBSET(8); PBSET(9); PBSET(10); PBSET(11); FENCE();
    VLD(12);
    PV(6); PBSET(12); PBSET(13); PBSET(14); PBSET(15); FENCE();
    VLD(13);
    PV(7);
    VLD(14);
    PV(8);
    VLD(15);
    PV(9);
    float fsc;
    {
      LWAIT(cm, 8u);
      const float mi = sm.wmax[(l >> 1) & 1][(l >> 2) & 7];
      float M = fmaxf(mi, rorf<4>(mi)); M = fmaxf(M, rorf<8>(M)); M = max16(M);
      fsc = __builtin_amdgcn_exp2f(mw - M);
      asm volatile("" : "+v"(fsc));
      FENCE();
    }
    PV(10); PV(11); PV(12); PV(13); PV(14); PV(15);
    {
      float* ap = &sm.acc[w - 8][(((l >> 1) & 1) * 8 + 2 * (l >> 4)) * 16 + (bank * 2 + (l & 1)) * 2];
      *(f32x2*)ap = f32x2{acc[0] * fsc, acc[1] * fsc};
      *(f32x2*)(ap + 16) = f32x2{acc[2] * fsc, acc[3] * fsc};
      if (w == 8) STAMP(11);
      if (w == 15) STAMP(25);
      WST(6);
      if (arrive(&sm.ca, l) == 7u) {
        f32x4 t[8];
#pragma unroll
        for (int i = 0; i < 8; ++i) t[i] = *(const f32x4*)&sm.acc[i][4 * l];
        const f32x4 t0 = ((t[0] + t[1]) + (t[2] + t[3])) + ((t[4] + t[5]) + (t[6] + t[7]));
        const u32x4 r = {nosent(t0[0]), nosent(t0[1]), nosent(t0[2]), nosent(t0[3])};
        __builtin_amdgcn_raw_buffer_store_b128(r, rWS, WS_OX + (u32)(g * 16 + (l >> 2)) * 1024u + j * 64 + (l & 3) * 16, 0, AUX_SC1);
        STAMP(12);
      }
      FENCE();
    }
  }
  FENCE();
  if (w == 8) DWLOADS();
  FENCE();
  float y[8];
  if (ow) {
    const int rs = l >> 3;
    u32 h0, h1, h2;
    for (u32 it = 0;; ++it) {
      h0 = lds_ld(&sm.hv[rs]); h1 = lds_ld(&sm.hv[8 + rs]); h2 = lds_ld(&sm.hv[16 + rs]);
      if (__all(h0 != SENT && h1 != SENT && h2 != SENT) || it >= SPIN_L) break;
      __builtin_amdgcn_s_sleep(1);
    }
#pragma unroll
    for (int e = 0; e < 4; ++e) {
      y[2 * e] = __uint_as_float(h0) * bflo(dw[0][e]) + __uint_as_float(h1) * bflo(dw[1][e]) + __uint_as_float(h2) * bflo(dw[2][e]);
      y[2 * e + 1] = __uint_as_float(h0) * bfhi(dw[0][e]) + __uint_as_float(h1) * bfhi(dw[1][e]) + __uint_as_float(h2) * bfhi(dw[2][e]);
    }
    if (w == 0) STAMP(13);
  }
  FENCE();
  if (!ow) {
    const int p0 = w - 9;
    u32 wb_;
    for (u32 it = 0;; ++it) {
      wb_ = lds_ld(&sm.wgt[(p0 + 7 * (l >> 4)) & 15]);
      if (__all(wb_ != SENT) || it >= SPIN_L) break;
      __builtin_amdgcn_s_sleep(1);
    }
    const unsigned char* op = ws + WS_OX + (u32)u * 1024u + (u32)p0 * 64u;
    u32 pend = p0 < 2 ? 7u : 3u;
    for (u32 it = 0; it < SPIN_G && pend; ++it) {
      u32x16 v[3];
      if (pend & 1u) asm volatile("s_load_dwordx16 %0, %1, 0x0 glc" : "=&s"(v[0]) : "s"(op) : "memory");
      if (pend & 2u) asm volatile("s_load_dwordx16 %0, %1, 0x1c0 glc" : "=&s"(v[1]) : "s"(op) : "memory");
      if (pend & 4u) asm volatile("s_load_dwordx16 %0, %1, 0x380 glc" : "=&s"(v[2]) : "s"(op) : "memory");
      asm volatile("s_waitcnt lgkmcnt(0)" ::: "memory");
#pragma unroll
      for (int k = 0; k < 3; ++k) {
        if (pend & (1u << k)) {
          asm volatile("" : "+s"(v[k]));
          u32 mx = v[k][0];
#pragma unroll
          for (int e = 1; e < 16; ++e) mx = mx > v[k][e] ? mx : v[k][e];
          if (mx != SENT) {
            pend &= ~(1u << k);
            u32 t = 0;
#pragma unroll
            for (int e = 0; e < 16; ++e) asm("v_writelane_b32 %0, %1, %2" : "+v"(t) : "s"(v[k][e]), "n"(16 * k + e));
            const u32 r = nosent(__uint_as_float(t) * __uint_as_float(wb_));
            const int p = p0 + 7 * k;
            if ((l >> 4) == k) lds_st(&sm.op[(((l >> 1) & 7) * 8 + (p >> 1)) * 4 + (l & 1) * 2 + (p & 1)], r);
          }
        }
      }
    }
    if (pend) { lds_st(&sm.op[4 * l], 0u); lds_st(&sm.op[4 * l + 1], 0u); lds_st(&sm.op[4 * l + 2], 0u); lds_st(&sm.op[4 * l + 3], 0u); }
    if (w == 9) STAMP(20);
    if (w == 13) STAMP(15);
  }
  if (ow) {
    float wf[16];
#pragma unroll
    for (int e = 0; e < 4; ++e) { wf[2 * e] = bflo(dw[3][e]); wf[2 * e + 1] = bfhi(dw[3][e]); wf[8 + 2 * e] = bflo(dw[4][e]); wf[9 + 2 * e] = bfhi(dw[4][e]); }
    if (w == 0) STAMP(21);
    u32x4 o4;
    for (u32 it = 0;; ++it) {
      o4 = lds_ld4(&sm.op[4 * l]);
      if (__all(o4[0] != SENT && o4[1] != SENT && o4[2] != SENT && o4[3] != SENT) || it >= SPIN_L) break;
    }
    float o0 = __uint_as_float(o4[0]) + __uint_as_float(o4[1]), o1 = __uint_as_float(o4[2]) + __uint_as_float(o4[3]);
    o0 += dpp<0xB1>(o0); o1 += dpp<0xB1>(o1);
    o0 += dpp<0x4E>(o0); o1 += dpp<0x4E>(o1);
    o0 += dpp<0x141>(o0); o1 += dpp<0x141>(o1);
#pragma unroll
    for (int e = 0; e < 8; ++e) y[e] += o0 * wf[e] + o1 * wf[8 + e];
    float a4[4], a2[2];
#pragma unroll
    for (int e = 0; e < 4; ++e) a4[e] = red32(y[e], y[e + 4]);
#pragma unroll
    for (int e = 0; e < 2; ++e) a2[e] = red16(a4[e], a4[e + 2]);
    const float t_ = (l & 8) ? a2[0] : a2[1], kp_ = (l & 8) ? a2[1] : a2[0];
    const float a1 = kp_ + dpp<0x128>(t_);
    __builtin_amdgcn_raw_buffer_store_b32(nosent(a1), rWS, WS_FX + (u32)(8 * w + (l & 7)) * 8192u + (u32)u * 32u +
                                          (4 * (l >> 5) + 2 * ((l >> 4) & 1) + ((l >> 3) & 1)) * 4, 0, AUX_SC1);
    if (w == 0) STAMP(14);
  }
  if (fcons) {
    const int pw = w & 7;
    if (w >= 9) {
      for (u32 it = 0; it < SPIN_L; ++it) {
        const u32x4 o4 = lds_ld4(&sm.op[4 * l]);
        if (__all(o4[0] != SENT && o4[1] != SENT && o4[2] != SENT && o4[3] != SENT)) break;
        __builtin_amdgcn_s_sleep(2);
      }
    }
#define FDELAY 4
    __builtin_amdgcn_s_sleep(FDELAY);
    if (w >= 8) __builtin_amdgcn_s_sleep(5);
    const u32 xo = *((lds_u16*)sm.xs + cidx * 8 + (l & 7));
    const u32 fb = WS_FX + cidx * 8192u + pw * 1024 + l * 16;
    u32x4 t;
    bool have = false;
    for (u32 it = 0; it < SPIN_G; ++it) {
      asm volatile("" ::: "memory");
      t = __builtin_amdgcn_raw_buffer_load_b128(rWS, fb, 0, AUX_SC1);
      if (__all(t[0] != SENT && t[1] != SENT && t[2] != SENT && t[3] != SENT)) { have = true; break; }
      if (lds_ld(&sm.fin[pw * 8]) != SENT) break;
    }
    if (have) {
      const float b0 = red32(__uint_as_float(t[0]), __uint_as_float(t[2])), b1 = red32(__uint_as_float(t[1]), __uint_as_float(t[3]));
      float c = red16(b0, b1);
      c += dpp<0x4E>(c); c += dpp<0x124>(c); c += dpp<0x128>(c);
      if ((l & 14) == 0) lds_st(&sm.fin[pw * 8 + 4 * (l & 1) + 2 * (l >> 5) + ((l >> 4) & 1)], nosent(c));
    }
    if (w == 0) STAMP(22);
    u32 fv;
    for (u32 it = 0;; ++it) {
      fv = lds_ld(&sm.fin[l]);
      if (__all(fv != SENT) || it >= SPIN_L) break;
    }
    float c = __uint_as_float(fv);
    c += dpp<0x128>(c); c = red16(c, c); c = red32(c, c);
    const __bf16 r = (__bf16)(bflo(xo) + c);
    if (l < 8) out[cidx * 8 + l] = __builtin_bit_cast(unsigned short, r);
    if (cidx == 0 && l == 0) __builtin_amdgcn_raw_buffer_store_b32(sm.ev + 1u, rWS, WS_E, 0, AUX_SC1);
    if (w == 0) STAMP(23);
  }
}

extern "C" void kernel_launch(void* const* d_in, const int* in_sizes, int n_in, void* d_out, int out_size, void* d_ws, size_t ws_size,
                              hipStream_t stream) {
  fused_decode<<<256, 1024, 0, stream>>>((const unsigned short*)d_in[0], (const unsigned short*)d_in[1], (const unsigned short*)d_in[2],
                                         (const unsigned short*)d_in[3], (const unsigned short*)d_in[4], (const unsigned short*)d_in[6],
                                         (const unsigned short*)d_in[7], (const unsigned short*)d_in[8], (unsigned char*)d_ws,
                                         (unsigned short*)d_out);
}
```

```cpp
#include <hip/hip_runtime.h>
#include <hip/hip_bf16.h>
#include <cstdint>

typedef unsigned u32;
typedef u32 u32x4 __attribute__((ext_vector_type(4)));
typedef u32 u32x2 __attribute__((ext_vector_type(2)));
typedef u32 u32x16 __attribute__((ext_vector_type(16)));
typedef float f32x4 __attribute__((ext_vector_type(4)));
typedef float f32x2 __attribute__((ext_vector_type(2)));
typedef __bf16 bf16x2 __attribute__((ext_vector_type(2)));
typedef __bf16 bf16x8 __attribute__((ext_vector_type(8)));
typedef __amdgpu_buffer_rsrc_t rsrc_t;

#define WIN 3

constexpr u32 WS_E = 0;
constexpr u32 WS_QX = 4096;
constexpr u32 WS_HX = WS_QX + 256 * 512;
constexpr u32 WS_FL = WS_HX + 256 * 1024;
#define OXS 64
constexpr u32 WS_AG = WS_FL + 256 * 128;
constexpr u32 WS_ML = WS_AG + 16 * 512;
constexpr u32 WS_OX = WS_ML + 256 * 128;
constexpr u32 WS_FX = WS_OX + 256 * 16 * OXS;
constexpr u32 WS_END = WS_FX + 36 * 16384;
constexpr u32 SPIN_G = 40000;
constexpr u32 SPIN_L = 4000000;
constexpr u32 SENT = 0xFFFFFFFFu;
constexpr u32 OOR = 0xFFF00000u;

#define STAMP(i) do { } while (0)
#define WST(id) do { } while (0)

#define AUX_NT 2
#define AUX_SC1 16
#define FENCE() __builtin_amdgcn_sched_barrier(0)
#define THR(n) do { asm volatile("s_waitcnt vmcnt(%0)" :: "n"(n) : "memory"); FENCE(); } while (0)
#define THROTTLE() THR(WIN - 1)
#define LDSWAIT() asm volatile("s_waitcnt lgkmcnt(0)" ::: "memory")

__device__ __forceinline__ float bflo(u32 u) { return __uint_as_float(u << 16); }
__device__ __forceinline__ float bfhi(u32 u) { return __uint_as_float(u & 0xffff0000u); }
template <int CTRL> __device__ __forceinline__ float dpp(float x) {
  return __int_as_float(__builtin_amdgcn_update_dpp(0, __float_as_int(x), CTRL, 0xf, 0xf, true));
}
template <int N> __device__ __forceinline__ float rorf(float x) {
  if constexpr ((N & 15) == 0) return x; else return dpp<0x120 + (N & 15)>(x);
}
template <int N> __device__ __forceinline__ u32 roru(u32 x) {
  if constexpr ((N & 15) == 0) return x; else return (u32)__builtin_amdgcn_update_dpp(0, (int)x, 0x120 + (N & 15), 0xf, 0xf, true);
}
__device__ __forceinline__ float red32(float a, float b) {
  auto r = __builtin_amdgcn_permlane32_swap(__float_as_uint(a), __float_as_uint(b), false, false);
  return __uint_as_float(r[0]) + __uint_as_float(r[1]);
}
__device__ __forceinline__ float red16(float a, float b) {
  auto r = __builtin_amdgcn_permlane16_swap(__float_as_uint(a), __float_as_uint(b), false, false);
  return __uint_as_float(r[0]) + __uint_as_float(r[1]);
}
__device__ __forceinline__ float max32(float a) {
  auto r = __builtin_amdgcn_permlane32_swap(__float_as_uint(a), __float_as_uint(a), false, false);
  return fmaxf(__uint_as_float(r[0]), __uint_as_float(r[1]));
}
__device__ __forceinline__ float max16(float a) {
  auto r = __builtin_amdgcn_permlane16_swap(__float_as_uint(a), __float_as_uint(a), false, false);
  return fmaxf(__uint_as_float(r[0]), __uint_as_float(r[1]));
}
__device__ __forceinline__ float rowsum16(float s) {
  s += dpp<0xB1>(s); s += dpp<0x4E>(s); s += dpp<0x141>(s); s += dpp<0x140>(s);
  return s;
}
__device__ __forceinline__ float rowmax16(float s) {
  s = fmaxf(s, dpp<0xB1>(s)); s = fmaxf(s, dpp<0x4E>(s)); s = fmaxf(s, dpp<0x141>(s)); s = fmaxf(s, dpp<0x140>(s));
  return s;
}
__device__ __forceinline__ float wavesum(float s) { s = rowsum16(s); s = red16(s, s); s = red32(s, s); return s; }
__device__ __forceinline__ rsrc_t mkr(const void* p, u32 n) { return __builtin_amdgcn_make_buffer_rsrc((void*)p, 0, n, 0x00020000); }
__device__ __forceinline__ u32x4 gran(float a, float b, u32 tag) { u32x4 r = {__float_as_uint(a), tag, __float_as_uint(b), tag}; return r; }
__device__ __forceinline__ bool tagok(u32x4 v, u32 tag) { return v[1] == tag && v[3] == tag; }
typedef __attribute__((address_space(3))) volatile u32 lds_u32;
typedef __attribute__((address_space(3))) volatile u32x4 lds_u32x4;
typedef __attribute__((address_space(3))) volatile unsigned short lds_u16;
__device__ __forceinline__ u32 lds_ld(u32* p) { return *(lds_u32*)p; }
__device__ __forceinline__ u32x4 lds_ld4(u32* p) { return *(lds_u32x4*)p; }
__device__ __forceinline__ void lds_st(u32* p, u32 v) { *(lds_u32*)p = v; }
#define PIN8(a) asm volatile("" : "+v"(a[0]), "+v"(a[1]), "+v"(a[2]), "+v"(a[3]), "+v"(a[4]), "+v"(a[5]), "+v"(a[6]), "+v"(a[7]))
__device__ __forceinline__ u32 nosent(float v) { const u32 t = __float_as_uint(v); return t == SENT ? 0x7FC00000u : t; }
__device__ __forceinline__ u32 arrive(u32* c, int l) {
  u32 old = 0;
  LDSWAIT();
  if (l == 0) old = __hip_atomic_fetch_add(c, 1u, __ATOMIC_RELAXED, __HIP_MEMORY_SCOPE_WORKGROUP);
  old = __builtin_amdgcn_readfirstlane(old);
  asm volatile("" ::: "memory");
  return old;
}
__device__ __forceinline__ f32x4 mfma(u32x4 a, u32x4 b, f32x4 c) {
  return __builtin_amdgcn_mfma_f32_16x16x32_bf16(__builtin_bit_cast(bf16x8, a), __builtin_bit_cast(bf16x8, b), c, 0, 0, 0);
}

struct __align__(16) Smem {
  float qp[8][64];
  float hs[12][64];
  float acc[8][256];
  float wmax[2][8];
  float wl[2][8];
  u32 qv[2][2][64];
  u32 xs[320];
  float xn[640];
  u32 op[256];
  u32 fin[16][16];
  u32 hv[24];
  u32 wgt[16];
  u32 cq, ch, cm, ca, fq, fx, cv, ff;
  u32 an, pad3, pad4, ev;
};
#define WA 3
#define NA 3
#define WB 6
#define NB 32
#define WC 6
#define NC 32
#define WD 6
#define BARI 2
#define TRIGN 8u
#define QSLP 4
#define HSLP 8
#define HSLP0 32
#define AUX_1ST 0
#define MSLP 4
#define MSLP0 16
#define AUX_E 0
#define WS_ 3
#define QPOS 7
#define TRIG 15
#define PACE_AT(base, T) do {                                                                                               \
      asm volatile("1: s_memrealtime s[94:95]\n s_waitcnt lgkmcnt(0)\n s_sub_u32 s94, s94, %0\n s_cmpk_lt_i32 s94, %1\n"    \
                   " s_cbranch_scc0 2f\n s_sleep 1\n s_branch 1b\n2:"                                                      \
                   :: "s"(base), "n"(T) : "s94", "s95", "scc", "memory");                                                   \
      FENCE(); } while (0)
#define PN0 3
#define PT0 2150
#define N1 4
#define N2 17
#define TAU1 285
#define TAU2 400
#define TAU3 285
constexpr int TK(int n) { return n <= N1 ? PT0 + (n - PN0) * TAU1 : n <= N2 ? TK(N1) + (n - N1) * TAU2 : TK(N2) + (n - N2) * TAU3; }
#define GT0 2600
#define GTAU 800
#define DOFF 300
#define DTAU 400
#define DSTAG 0
#define AOFF 0
#define ACL 100
#define ANCHOR(dst) do { const u32 a_ = lds_ld(&sm.an);                                                                     \
      dst = __builtin_amdgcn_readfirstlane(a_ ? tb + (u32)min(max((int)(a_ - tb), -ACL), ACL) : tb); FENCE(); } while (0)
#define BUMP(c) do { if (l == 0) __hip_atomic_fetch_add(&sm.c, 1u, __ATOMIC_RELAXED, __HIP_MEMORY_SCOPE_WORKGROUP); } while (0)
#define LWAIT(c, n) do { for (u32 it = 0; lds_ld(&sm.c) != (n) && it < SPIN_L; ++it) __builtin_amdgcn_s_sleep(1); asm volatile("" ::: "memory"); } while (0)

__global__ void __launch_bounds__(1024, 4)
fused_decode(const unsigned short* __restrict__ x, const unsigned short* __restrict__ Kc, const unsigned short* __restrict__ Vc,
             const unsigned short* __restrict__ wq, const unsigned short* __restrict__ wo, const unsigned short* __restrict__ wg,
             const unsigned short* __restrict__ wu, const unsigned short* __restrict__ wd, unsigned char* __restrict__ ws,
             unsigned short* __restrict__ out) {
  __shared__ Smem sm;
  const int b = blockIdx.x;
  const int w = __builtin_amdgcn_readfirstlane(threadIdx.x >> 6);
  const int l = threadIdx.x & 63;
#define PXA 0
#define PXB 0
  const int u = ((b + PXA) & 7) * 32 + ((b >> 3) ^ PXB);
  const int g = u >> 4, j = u & 15, k4 = u & 3;
  const bool fcons = b < 36;
  const u32 cidx = (u32)b;
  const u32 tb = (u32)__builtin_amdgcn_s_memrealtime();
  FENCE();
  asm volatile("" :: "s"(x), "s"(Kc), "s"(Vc), "s"(wq), "s"(wo), "s"(wg), "s"(wu), "s"(wd), "s"(ws));
  const rsrc_t rWS = mkr(ws, WS_END);
  const bool ow = w < 9;
  const rsrc_t rD = mkr(wd, 4608u * 1152u), rO = mkr(wo, 4096u * 1152u);
  const u32 co = (u32)w * 128u + (l & 7) * 16;
  const u32 od = ow ? (u32)(18 * u + (l >> 3)) * 1152u + co : OOR;
  const u32 oo = ow ? (u32)(16 * u + (l >> 3)) * 1152u + co : OOR;
  u32x4 dw[5];
#define DWLOADS() do {                                                                                     \
    dw[0] = __builtin_amdgcn_raw_buffer_load_b128(rD, od, 0, AUX_NT);                                      \
    dw[1] = __builtin_amdgcn_raw_buffer_load_b128(rD, od, 8 * 1152, AUX_NT);                               \
    dw[2] = __builtin_amdgcn_raw_buffer_load_b128(rD, l < 16 ? od : OOR, 16 * 1152, AUX_NT);               \
    dw[3] = __builtin_amdgcn_raw_buffer_load_b128(rO, oo, 0, AUX_NT);                                      \
    dw[4] = __builtin_amdgcn_raw_buffer_load_b128(rO, oo, 8 * 1152, AUX_NT); FENCE(); } while (0)

  if (w < 8) {
    const u32 ts0 = tb + (u32)(w * (GTAU / 60));
    const rsrc_t rX = mkr(x, 1152), rQ = mkr(wq, 576u * 8192u), rG = mkr((g & 1) ? wu : wg, 576u * 9216u);
    const int rb = 144 * k4 + 18 * w;
    const u32 qo = (u32)(rb + (l >> 3)) * 8192u + (u32)(256 * g + 64 * (j >> 2)) * 2 + (l & 7) * 16;
    const int uB = 9 * j + 3 * (w >> 1) + 1, uA = uB - 1 + 2 * (w & 1);
    const int rA = 36 * (uA & 15), rB = 36 * (uB & 15) + 18 * (w & 1);
    const u32 gv = w < 6 ? (u32)(l >> 3) * 9216u + (u32)(u >> 5) * 1152u + (l & 7) * 16 : OOR;
    const u32 gB = w < 6 ? (u32)(rB * 9216 + (uB >> 4) * 128) : 0u, gA = (u32)(rA * 9216 + (uA >> 4) * 128);
#define AUX_G AUX_NT
    u32x4 xa = {0u, 0u, 0u, 0u};
    u32 xb = 0u, Ev = 0u;
    if (w == 0) {
      xa = __builtin_amdgcn_raw_buffer_load_b128(rX, l * 16, 0, 0);
      xb = __builtin_amdgcn_raw_buffer_load_b32(rX, 1024 + l * 4, 0, 0);
      Ev = __builtin_amdgcn_raw_buffer_load_b32(rWS, WS_E, 0, AUX_E);
    }
    {
      const u32x4 sv = {SENT, SENT, SENT, SENT};
      if (w == 7) __builtin_amdgcn_raw_buffer_store_b128(sv, rWS, WS_OX + (u32)u * (16 * OXS) + (l >> 2) * OXS + (l & 3) * 16, 0, AUX_SC1);
      if (w == 6) __builtin_amdgcn_raw_buffer_store_b128(sv, rWS, l < 8 ? WS_ML + (u32)u * 128u + l * 16 : OOR, 0, AUX_SC1);
    }
    FENCE();
    u32x4 fq_[3], fg[7];
    fq_[0] = __builtin_amdgcn_raw_buffer_load_b128(rQ, qo, 0, AUX_NT); FENCE();
    fq_[1] = __builtin_amdgcn_raw_buffer_load_b128(rQ, qo, 8 * 8192, AUX_NT); FENCE();
    fq_[2] = __builtin_amdgcn_raw_buffer_load_b128(rQ, l < 16 ? qo : OOR, 16 * 8192, AUX_NT); FENCE();
    if (fcons) {
      const u32x4 sv = {SENT, SENT, SENT, SENT};
#pragma unroll
      for (int i = 0; i < 2; ++i) __builtin_amdgcn_raw_buffer_store_b128(sv, rWS, WS_FX + cidx * 16384u + (u32)w * 2048u + l * 16, i * 1024, AUX_SC1);
    }
    FENCE();
    if (threadIdx.x < 281) {
      const u32 iv = threadIdx.x < 276 ? SENT : 0u;
      *(u32x2*)&sm.op[2 * threadIdx.x] = u32x2{iv, iv};
    }
    LDSWAIT(); __builtin_amdgcn_s_barrier(); FENCE(); WST(0);
    fg[0] = __builtin_amdgcn_raw_buffer_load_b128(rG, gv, gB, AUX_G); FENCE();
    if (w == 0) {
      float xf[10];
#pragma unroll
      for (int e = 0; e < 4; ++e) { xf[2 * e] = bflo(xa[e]); xf[2 * e + 1] = bfhi(xa[e]); }
      xf[8] = bflo(xb); xf[9] = bfhi(xb);
      const float mean = wavesum((((xf[0] + xf[1]) + (xf[2] + xf[3])) + ((xf[4] + xf[5]) + (xf[6] + xf[7]))) + (xf[8] + xf[9])) * (1.0f / 576.0f);
#pragma unroll
      for (int e = 0; e < 10; ++e) xf[e] -= mean;
      float v0 = xf[0] * xf[0], v1 = xf[1] * xf[1];
#pragma unroll
      for (int e = 2; e < 8; e += 2) { v0 += xf[e] * xf[e]; v1 += xf[e + 1] * xf[e + 1]; }
      v0 += l < 32 ? xf[8] * xf[8] + xf[9] * xf[9] : 0.f;
      const float rstd = __builtin_amdgcn_rsqf(wavesum(v0 + v1) * (1.0f / 576.0f) + 1e-5f);
      *(f32x4*)&sm.xn[8 * l] = f32x4{xf[0] * rstd, xf[1] * rstd, xf[2] * rstd, xf[3] * rstd};
      *(f32x4*)&sm.xn[8 * l + 4] = f32x4{xf[4] * rstd, xf[5] * rstd, xf[6] * rstd, xf[7] * rstd};
      *(f32x2*)&sm.xn[512 + 2 * l] = f32x2{xf[8] * rstd, xf[9] * rstd};
      *(u32x4*)&sm.xs[4 * l] = xa;
      sm.xs[256 + l] = xb;
      if (l == 0) sm.ev = Ev;
      LDSWAIT();
      if (l == 0) lds_st(&sm.fx, 1u);
      STAMP(2);
    }
    FENCE();
    LWAIT(fx, 1u);
    const u32 tag = __builtin_amdgcn_readfirstlane(sm.ev) * 0x9E3779B1u + 0x7F4A7C15u;
    float xq[3], xg[8];
#pragma unroll
    for (int i = 0; i < 3; ++i) xq[i] = sm.xn[rb + (l >> 3) + 8 * i];
    asm volatile("" : "+v"(xq[0]), "+v"(xq[1]), "+v"(xq[2]));
    FENCE();
    THR(1);
    WST(1);
#define FACC(N, G, F, X) do {                                                                                          \
      _Pragma("unroll") for (int e = 0; e < 4; ++e) G[e] = f32x2{X[0], X[0]} * f32x2{bflo(F[0][e]), bfhi(F[0][e])};    \
      _Pragma("unroll") for (int i = 1; i < N; ++i)                                                                    \
        _Pragma("unroll") for (int e = 0; e < 4; ++e) G[e] += f32x2{X[i], X[i]} * f32x2{bflo(F[i][e]), bfhi(F[i][e])}; \
    } while (0)
    {
      f32x2 g2[4];
      FACC(3, g2, fq_, xq);
      float a4[4], a2[2];
#pragma unroll
      for (int e = 0; e < 4; ++e) a4[e] = red32(g2[e >> 1][e & 1], g2[2 + (e >> 1)][e & 1]);
#pragma unroll
      for (int e = 0; e < 2; ++e) a2[e] = red16(a4[e], a4[e + 2]);
      const float t_ = (l & 8) ? a2[0] : a2[1], kp_ = (l & 8) ? a2[1] : a2[0];
      sm.qp[w][8 * (l & 7) + 4 * (l >> 5) + 2 * ((l >> 4) & 1) + ((l >> 3) & 1)] = kp_ + dpp<0x128>(t_);
      if (w == 0) STAMP(1);
      if (arrive(&sm.cq, l) == 7u) {
        const int P_ = l & 31, hf = l >> 5;
        const f32x2 s_ = (*(const f32x2*)&sm.qp[hf * 4][2 * P_] + *(const f32x2*)&sm.qp[hf * 4 + 1][2 * P_]) +
                         (*(const f32x2*)&sm.qp[hf * 4 + 2][2 * P_] + *(const f32x2*)&sm.qp[hf * 4 + 3][2 * P_]);
        const float s0 = red32(s_[0], s_[0]), s1 = red32(s_[1], s_[1]);
        if (l < 32) __builtin_amdgcn_raw_buffer_store_b128(gran(s0, s1, tag), rWS, WS_QX + (u32)u * 512u + P_ * 16, 0, AUX_SC1);
        __builtin_amdgcn_raw_buffer_store_b32(tag, rWS, l < 16 ? WS_FL + (u32)(g * 16 + l) * 128u + j * 4 : OOR, 0, AUX_SC1);
        if (fcons) __builtin_amdgcn_raw_buffer_store_b64(u32x2{tb, tag}, rWS, l < 16 ? WS_AG + (u32)l * 512u + cidx * 8u : OOR, 0, AUX_SC1);
        STAMP(3);
      }
      FENCE();
    }
    if (w < 6) {
      const float* const xr = &sm.xn[l >> 3];
      xg[0] = xr[rB]; xg[1] = xr[rB + 8];
      xg[2] = xr[l < 16 ? rB + 16 : rA - 2];
      xg[4] = xr[rA + 6]; xg[5] = xr[rA + 14]; xg[6] = xr[rA + 22]; xg[7] = xr[rA + 30];
      xg[7] = l < 48 ? xg[7] : 0.f;
      xg[3] = l < 16 ? 0.f : xg[2];
      xg[2] = l < 16 ? xg[2] : 0.f;
      const int c0 = (9 * j) >> 4, n0 = 16 - ((9 * j) & 15);
      const int s0 = j - ((c0 * 455) >> 8);
      const int c1 = n0 < 9 ? c0 + 1 : j == 0 ? 0 : j == 6 ? 4 : 8, s1 = n0 < 9 ? 0 : 2;
      const u32 cA = (u32)(64 * c0 + l), cB = (u32)(64 * c1 + l), qA = (cA * 3641u) >> 16, qB = (cB * 3641u) >> 16;
      const u32 hbase = WS_HX + (u32)(u & ~31) * 1024u + (u32)(g & 1) * 432u;
      u32 hA = hbase + (u32)s0 * 144u + qA * 1024u + (cA - 18u * qA) * 8u;
      u32 hB = (n0 < 9 || j == 0 || j == 6 || j == 15) ? hbase + (u32)s1 * 144u + qB * 1024u + (cB - 18u * qB) * 8u : OOR;
      u32 gv2 = gv + (l < 16 ? gB + 16u * 9216u : gA - 2u * 9216u), gv6 = l < 48 ? gv : OOR;
      PIN8(xg);
      asm volatile("" : "+v"(hA), "+v"(hB), "+v"(gv2), "+v"(gv6));
      const int cl = 8 * (l & 7) + 4 * (l >> 5) + 2 * ((l >> 4) & 1) + ((l >> 3) & 1);
#define GLD(k, vo, so) do { PACE_AT(ts0, (GT0 + ((k) - 1) * GTAU) / 10); fg[k] = __builtin_amdgcn_raw_buffer_load_b128(rG, vo, so, AUX_G); FENCE(); } while (0)
#define COLSUM(G) ({ float a4_[4], a2_[2];                                                                              \
      _Pragma("unroll") for (int e = 0; e < 4; ++e) a4_[e] = red32(G[e >> 1][e & 1], G[2 + (e >> 1)][e & 1]);           \
      _Pragma("unroll") for (int e = 0; e < 2; ++e) a2_[e] = red16(a4_[e], a4_[e + 2]);                                 \
      const float t_ = (l & 8) ? a2_[0] : a2_[1], kp_ = (l & 8) ? a2_[1] : a2_[0];                                      \
      kp_ + dpp<0x128>(t_); })
      GLD(1, gv, gB + 8u * 9216u); GLD(2, gv2, 0); GLD(3, gv, gA + 6u * 9216u); GLD(4, gv, gA + 14u * 9216u);
      {
        f32x2 g2[4];
        FACC(3, g2, fg, xg);
        sm.hs[2 * (w & 6) + 1 + (w & 1)][cl] = COLSUM(g2);
        FENCE();
      }
      GLD(5, gv, gA + 22u * 9216u); GLD(6, gv6, gA + 30u * 9216u);
      {
        f32x2 g2[4];
        FACC(5, g2, (fg + 2), (xg + 3));
        sm.hs[2 * (w & 6) + 3 * (w & 1)][cl] = COLSUM(g2);
        WST(2);
        if (arrive(&sm.ch, l) == 5u) {
          float t[12], a0 = 0.f, a1 = 0.f;
#pragma unroll
          for (int i = 0; i < 12; ++i) t[i] = sm.hs[i][l];
#pragma unroll
          for (int i = 0; i < 12; ++i) {
            const bool first = 3 * (i >> 2) + ((i & 3) + 1) / 2 < n0;
            a0 += first ? t[i] : 0.f; a1 += first ? 0.f : t[i];
          }
          const u32x2 r0 = {__float_as_uint(a0), tag}, r1 = {__float_as_uint(a1), tag};
          __builtin_amdgcn_raw_buffer_store_b64(r0, rWS, hA, 0, AUX_SC1);
          __builtin_amdgcn_raw_buffer_store_b64(r1, rWS, hB, 0, AUX_SC1);
          STAMP(6);
        }
        FENCE();
      }
    } else {
      if (w == 6) {
        const unsigned char* fp = ws + WS_FL + (u32)u * 128u;
        LWAIT(cq, 8u);
        for (u32 it = 0; it < SPIN_G; ++it) {
          u32x16 v;
          __builtin_amdgcn_s_sleep(QSLP);
          asm volatile("s_load_dwordx16 %0, %1, 0x0 glc\n s_waitcnt lgkmcnt(0)" : "=&s"(v) : "s"(fp) : "memory");
          u32 bad = 0;
#pragma unroll
          for (int e = 0; e < 16; ++e) bad |= v[e] ^ tag;
          if (bad == 0) break;
        }
        if (l == 0) lds_st(&sm.ff, 1u);
      } else {
        LWAIT(ff, 1u);
      }
      if (w == 6) STAMP(4);
      const u32 g1a = WS_QX + (u32)(g * 16 + (w - 6) * 8 + (l >> 4)) * 512u + (l & 15) * 16;
      u32x4 g1[4];
#pragma unroll
      for (int i = 0; i < 4; ++i) g1[i] = __builtin_amdgcn_raw_buffer_load_b128(rWS, g1a + (i & 1) * 256 + (i >> 1) * 2048, 0, AUX_1ST);
      asm volatile("" : "+v"(g1[0]), "+v"(g1[1]), "+v"(g1[2]), "+v"(g1[3]));
      if (w == 6) STAMP(16);
      if (!__all(tagok(g1[0], tag) && tagok(g1[1], tag) && tagok(g1[2], tag) && tagok(g1[3], tag))) {
        for (u32 it = 0;; ++it) {
          asm volatile("" ::: "memory");
#pragma unroll
          for (int i = 0; i < 4; ++i) g1[i] = __builtin_amdgcn_raw_buffer_load_b128(rWS, g1a + (i & 1) * 256 + (i >> 1) * 2048, 0, AUX_SC1);
          asm volatile("" : "+v"(g1[0]), "+v"(g1[1]), "+v"(g1[2]), "+v"(g1[3]));
          if (__all(tagok(g1[0], tag) && tagok(g1[1], tag) && tagok(g1[2], tag) && tagok(g1[3], tag)) || it >= SPIN_G) break;
        }
      }
      const float qs = 0.08838834764831845f * 1.4426950408889634f;
#pragma unroll
      for (int i = 0; i < 4; ++i) {
        float a = red16(__uint_as_float(g1[i][0]), __uint_as_float(g1[i][2]));
        a = red32(a, a) * qs;
        const u32 hib = __builtin_bit_cast(unsigned short, (__bf16)a);
        const u32 lob = __builtin_bit_cast(unsigned short, (__bf16)(a - bflo(hib)));
        *((lds_u16*)&sm.qv[w - 6][l >> 5][16 * i + (l & 15)] + ((l >> 4) & 1)) = (unsigned short)(l < 32 ? hib : lob);
      }
      LDSWAIT();
      BUMP(fq);
      if (w == 6) STAMP(5);
      WST(3);
    }
    FENCE();
    if (w == 7) {
      {
        const u32 ab = l < 18 ? WS_AG + (u32)g * 512u + l * 16 : OOR;
        u32x4 e0;
        for (u32 it = 0;; ++it) {
          asm volatile("" ::: "memory");
          e0 = __builtin_amdgcn_raw_buffer_load_b128(rWS, ab, 0, AUX_SC1);
          if (__all(l >= 18 || tagok(e0, tag)) || it >= SPIN_G) break;
          __builtin_amdgcn_s_sleep(8);
        }
        const int d0 = min(max((int)(e0[0] - tb), -200), 200), d1 = min(max((int)(e0[2] - tb), -200), 200);
        const float dm = rowsum16((float)(d0 + d1)) * (1.0f / 32.0f);
        if (l == 0) lds_st(&sm.an, (tb + (u32)((int)(dm + 256.5f) - 256 + AOFF)) | 1u);
        STAMP(19);
      }
      const bool on = (l & 15) < 9 && l < 48;
      const u32 hb = on ? WS_HX + (u32)u * 1024u + (u32)(l >> 4) * 144u + (l & 15) * 16 : OOR;
      const unsigned char* hs = ws + WS_HX + (u32)u * 1024u;
      LWAIT(ch, 6u);
      for (u32 it = 0; it < SPIN_G; ++it) {
        u32 a0, a6; u32x4 a1, a2, a3, a4, a5;
        __builtin_amdgcn_s_sleep(HSLP);
        asm volatile("s_load_dword %0, %7, 0x4 glc\n s_load_dwordx4 %1, %7, 0x8c glc\n s_load_dwordx4 %2, %7, 0x11c glc\n s_load_dwordx4 %3, %7, 0x1ac glc\n"
                     " s_load_dwordx4 %4, %7, 0x23c glc\n s_load_dwordx4 %5, %7, 0x2cc glc\n s_load_dword %6, %7, 0x35c glc\n s_waitcnt lgkmcnt(0)"
                     : "=&s"(a0), "=&s"(a1), "=&s"(a2), "=&s"(a3), "=&s"(a4), "=&s"(a5), "=&s"(a6) : "s"(hs) : "memory");
        if (((a0 ^ tag) | (a6 ^ tag) | (a1[0] ^ tag) | (a1[2] ^ tag) | (a2[0] ^ tag) | (a2[2] ^ tag) | (a3[0] ^ tag) | (a3[2] ^ tag) |
             (a4[0] ^ tag) | (a4[2] ^ tag) | (a5[0] ^ tag) | (a5[2] ^ tag)) == 0) break;
      }
      u32x4 t0, t1;
      for (u32 it = 0;; ++it) {
        asm volatile("" ::: "memory");
        t0 = __builtin_amdgcn_raw_buffer_load_b128(rWS, hb, 0, AUX_SC1);
        t1 = __builtin_amdgcn_raw_buffer_load_b128(rWS, hb + 432, 0, AUX_SC1);
        if (__all(!on || (tagok(t0, tag) && tagok(t1, tag))) || it >= SPIN_G) break;
        __builtin_amdgcn_s_sleep(8);
      }
      float gs = red16(__uint_as_float(t0[0]), __uint_as_float(t0[2])), us = red16(__uint_as_float(t1[0]), __uint_as_float(t1[2]));
      gs = red32(gs, gs); us = red32(us, us);
      const float hval = on ? gs / (1.0f + __expf(-gs)) * us : 0.f;
      if (l < 32 && (l & 15) < 12) lds_st(&sm.hv[2 * (l & 15) + (l >> 4)], nosent(hval));
      STAMP(10);
    } else if (w == 6) {
      LWAIT(cm, 8u);
      const int h = (l >> 3) & 1;
      const float mi = sm.wmax[h][l & 7], li = sm.wl[h][l & 7];
      float M = fmaxf(mi, dpp<0xB1>(mi)); M = fmaxf(M, dpp<0x4E>(M)); M = fmaxf(M, dpp<0x141>(M));
      float L = li * __builtin_amdgcn_exp2f(mi - M);
      L += dpp<0xB1>(L); L += dpp<0x4E>(L); L += dpp<0x141>(L);
      const u32x2 ml = {nosent(M), nosent(L)};
      __builtin_amdgcn_raw_buffer_store_b64(ml, rWS, l < 16 ? WS_ML + (u32)(g * 16 + l) * 128u + j * 8 : OOR, 0, AUX_SC1);
      STAMP(17);
      __builtin_amdgcn_s_sleep(MSLP0);
      const unsigned char* mp = ws + WS_ML + (u32)u * 128u;
      u32x16 v[2];
      for (u32 it = 0; it < SPIN_G; ++it) {
        asm volatile("s_load_dwordx16 %0, %1, 0x0 glc" : "=&s"(v[0]) : "s"(mp) : "memory");
        asm volatile("s_load_dwordx16 %0, %1, 0x40 glc" : "=&s"(v[1]) : "s"(mp) : "memory");
        asm volatile("s_waitcnt lgkmcnt(0)" ::: "memory");
        u32 mx = 0;
#pragma unroll
        for (int k = 0; k < 2; ++k) {
          asm volatile("" : "+s"(v[k]));
#pragma unroll
          for (int e = 0; e < 16; ++e) mx = mx > v[k][e] ? mx : v[k][e];
        }
        if (mx != SENT) break;
        __builtin_amdgcn_s_sleep(MSLP);
      }
      u32 tm = 0, tl = 0;
#pragma unroll
      for (int k = 0; k < 2; ++k)
#pragma unroll
        for (int e = 0; e < 8; ++e) {
          asm("v_writelane_b32 %0, %1, %2" : "+v"(tm) : "s"(v[k][2 * e]), "n"(8 * k + e));
          asm("v_writelane_b32 %0, %1, %2" : "+v"(tl) : "s"(v[k][2 * e + 1]), "n"(8 * k + e));
        }
      asm volatile("s_nop 1" : "+v"(tm), "+v"(tl));
      const float mi2 = __uint_as_float(tm);
      const float e = __builtin_amdgcn_exp2f(mi2 - rowmax16(mi2));
      const float L2 = rowsum16(e * __uint_as_float(tl));
      if (l < 16) lds_st(&sm.wgt[l], nosent(e / L2));
      STAMP(18);
    }
    FENCE();
    for (u32 it = 0; lds_ld(&sm.cv) < TRIGN && it < SPIN_L; ++it) __builtin_amdgcn_s_sleep(1);
    asm volatile("" ::: "memory");
    if (w == 0) STAMP(24);
    u32 td0;
    ANCHOR(td0);
    td0 += (u32)(w * (DSTAG / 10));
#define DPACE(k) PACE_AT(td0, (TK(31) + DOFF + (k) * DTAU) / 10)
    DPACE(0); dw[0] = __builtin_amdgcn_raw_buffer_load_b128(rD, od, 0, AUX_NT); FENCE();
    DPACE(1); dw[1] = __builtin_amdgcn_raw_buffer_load_b128(rD, od, 8 * 1152, AUX_NT); FENCE();
    DPACE(2); dw[3] = __builtin_amdgcn_raw_buffer_load_b128(rO, oo, 0, AUX_NT); FENCE();
    DPACE(3); dw[4] = __builtin_amdgcn_raw_buffer_load_b128(rO, oo, 8 * 1152, AUX_NT); FENCE();
    dw[2] = __builtin_amdgcn_raw_buffer_load_b128(rD, l < 16 ? od : OOR, 16 * 1152, AUX_NT); FENCE();
    if (w == 0) STAMP(7);
    FENCE();
  } else {
    const rsrc_t rK = mkr(Kc, 33554432u), rV = mkr(Vc, 33554432u);
#define PKV 0
#define KVI 0
    const u32 kvb = (u32)(u ^ PKV) * 131072u + (u32)(w - 8) * 16384u;
#define TVO(i) (((i) & 3) * 1024)
#define TSO(i) (((i) >> 2) * 4096)
    const u32 voff = kvb + (u32)l * 16u;
    const u32 koff = kvb + (u32)(((l >> 2) & 3) * 256 + (l >> 4) * 64 + (l & 3) * 16);
    u32x4 kk[16], vv[16];
#define WINOF(n) ((n) < NA ? WA : (n) < NB ? WB : (n) < NC ? WC : WD)
    u32 tk = tb;
#define PACE(n) do { if ((n) >= PN0) PACE_AT(tk, TK(n) / 10); } while (0)
#define KLD(i) do { if ((i) >= WA) THR(WINOF(i) - 1);                                                                      \
      PACE(i);                                                                                                              \
      kk[i] = __builtin_amdgcn_raw_buffer_load_b128(rK, koff + TVO(i), TSO(i), AUX_NT); FENCE();                            \
      if ((i) == BARI) { __builtin_amdgcn_s_barrier(); FENCE(); WST(0); } } while (0)
#define VLD(i) do { if ((i) % 3 == 1 && (i) < 12) ANCHOR(tk); THR(WINOF(16 + (i)) - 1);                                                                               \
      PACE(16 + (i));                                                                                                       \
      vv[i] = __builtin_amdgcn_raw_buffer_load_b128(rV, voff + TVO(i), TSO(i), AUX_NT); FENCE();                            \
      if ((i) == TRIG) { BUMP(cv); FENCE(); } } while (0)
    KLD(0); KLD(1); KLD(2); KLD(3); KLD(4); KLD(5); KLD(6); KLD(7);
    WST(1);
    KLD(8); KLD(9); KLD(10); KLD(11); KLD(12); KLD(13); KLD(14); KLD(15);
    WST(2);
    VLD(0); VLD(1); VLD(2); VLD(3); VLD(4); VLD(5);
    if (QPOS >= 6) VLD(6);
    if (QPOS >= 7) VLD(7);
    WST(3);
    LWAIT(fq, 2u);
    if (w == 8) STAMP(8);
    WST(4);
    const int bank = (l >> 2) & 3;
    u32 pk[4];
    float mw;
    {
      const u32x4 qb = *(const u32x4*)&sm.qv[(l >> 1) & 1][l & 1][4 * ((l >> 4) * 4 + bank)];
      float sc[4];
#pragma unroll
      for (int ab = 0; ab < 4; ++ab) {
        const f32x4 z = {0.f, 0.f, 0.f, 0.f};
        const f32x4 d0 = mfma(kk[4 * ab], qb, z), d1 = mfma(kk[4 * ab + 1], qb, z), d2 = mfma(kk[4 * ab + 2], qb, z), d3 = mfma(kk[4 * ab + 3], qb, z);
        float a0 = ((d0[0] + rorf<-4>(d0[1])) + rorf<-8>(d0[2])) + rorf<-12>(d0[3]);
        float a1 = ((d1[1] + rorf<4>(d1[0])) + rorf<-4>(d1[2])) + rorf<-8>(d1[3]);
        float a2 = ((d2[2] + rorf<8>(d2[0])) + rorf<4>(d2[1])) + rorf<-4>(d2[3]);
        float a3 = ((d3[3] + rorf<12>(d3[0])) + rorf<8>(d3[1])) + rorf<4>(d3[2]);
        asm volatile("" : "+v"(a0), "+v"(a1), "+v"(a2), "+v"(a3));
        const float t01 = (l & 4) ? a1 : a0, t23 = (l & 4) ? a3 : a2;
        const float t = (l & 8) ? t23 : t01;
        sc[ab] = t + dpp<0xB1>(t);
        asm volatile("" : "+v"(sc[ab]));
        if (ab == 1) { FENCE(); VLD(QPOS + 1); }
      }
      FENCE(); VLD(QPOS + 2);
      float m = fmaxf(fmaxf(sc[0], sc[1]), fmaxf(sc[2], sc[3]));
      m = fmaxf(m, rorf<4>(m)); m = fmaxf(m, rorf<8>(m));
      m = max32(max16(m));
      mw = m;
#pragma unroll
      for (int ab = 0; ab < 4; ++ab) {
        const float p = __builtin_amdgcn_exp2f(sc[ab] - m);
        const bf16x2 t = {(__bf16)p, (__bf16)p};
        pk[ab] = __builtin_bit_cast(u32, t);
      }
      float lt = (bfhi(pk[0]) + bfhi(pk[1])) + (bfhi(pk[2]) + bfhi(pk[3]));
      lt += rorf<4>(lt); lt += rorf<8>(lt);
      lt = red16(lt, lt); lt = red32(lt, lt);
      if ((l & 61) == 0) { sm.wmax[l >> 1][w - 8] = mw; sm.wl[l >> 1][w - 8] = lt; }
      LDSWAIT();
      BUMP(cm);
    }
    if (w == 8) STAMP(9);
    WST(5);
    FENCE();
    const u32 hm = (l & 1) ? 0xffff0000u : 0x0000ffffu;
    const u32 m0 = bank == 0 ? hm : 0u, m1 = bank == 1 ? hm : 0u, m2 = bank == 2 ? hm : 0u, m3 = bank == 3 ? hm : 0u;
    u32x4 pb[16];
#define PBSET(t) do { pb[t][0] = roru<4 * (0 - ((t) & 3))>(pk[(t) >> 2]) & m0; pb[t][1] = roru<4 * (1 - ((t) & 3))>(pk[(t) >> 2]) & m1;   \
                      pb[t][2] = roru<4 * (2 - ((t) & 3))>(pk[(t) >> 2]) & m2; pb[t][3] = roru<4 * (3 - ((t) & 3))>(pk[(t) >> 2]) & m3;   \
                      asm volatile("" : "+v"(pb[t])); } while (0)
    f32x4 acc = {0.f, 0.f, 0.f, 0.f};
#define PV(i) do { acc = mfma(vv[i], pb[i], acc); asm volatile("" : "+v"(acc)); FENCE(); } while (0)
    if (QPOS < 6) VLD(8);
    if (QPOS < 7) VLD(9);
    PBSET(0); PBSET(1); PBSET(2); PBSET(3); PV(0); PV(1); PV(2); PV(3);
    VLD(10);
    PBSET(4); PBSET(5); PBSET(6); PBSET(7); PV(4);
    VLD(11);
    PV(5); PBSET(8); PBSET(9); PBSET(10); PBSET(11); FENCE();
    VLD(12);
    PV(6); PBSET(12); PBSET(13); PBSET(14); PBSET(15); FENCE();
    VLD(13);
    PV(7);
    VLD(14);
    PV(8);
    VLD(15);
    PV(9);
    float fsc;
    {
      LWAIT(cm, 8u);
      const float mi = sm.wmax[(l >> 1) & 1][(l >> 2) & 7];
      float M = fmaxf(mi, rorf<4>(mi)); M = fmaxf(M, rorf<8>(M)); M = max16(M);
      fsc = __builtin_amdgcn_exp2f(mw - M);
      asm volatile("" : "+v"(fsc));
      FENCE();
    }
    PV(10); PV(11); PV(12); PV(13); PV(14); PV(15);
    {
      float* ap = &sm.acc[w - 8][(((l >> 1) & 1) * 8 + 2 * (l >> 4)) * 16 + (bank * 2 + (l & 1)) * 2];
      *(f32x2*)ap = f32x2{acc[0] * fsc, acc[1] * fsc};
      *(f32x2*)(ap + 16) = f32x2{acc[2] * fsc, acc[3] * fsc};
      if (w == 8) STAMP(11);
      if (w == 15) STAMP(25);
      WST(6);
      if (arrive(&sm.ca, l) == 7u) {
        f32x4 t[8];
#pragma unroll
        for (int i = 0; i < 8; ++i) t[i] = *(const f32x4*)&sm.acc[i][4 * l];
        const f32x4 t0 = ((t[0] + t[1]) + (t[2] + t[3])) + ((t[4] + t[5]) + (t[6] + t[7]));
        const u32x4 r = {nosent(t0[0]), nosent(t0[1]), nosent(t0[2]), nosent(t0[3])};
        __builtin_amdgcn_raw_buffer_store_b128(r, rWS, WS_OX + (u32)(g * 16 + (l >> 2)) * (16 * OXS) + j * OXS + (l & 3) * 16, 0, AUX_SC1);
        STAMP(12);
      }
      FENCE();
    }
  }
  FENCE();
  if (w == 8) DWLOADS();
  FENCE();
  float y[8];
  if (ow) {
    const int rs = l >> 3;
    u32 h0, h1, h2;
    for (u32 it = 0;; ++it) {
      h0 = lds_ld(&sm.hv[rs]); h1 = lds_ld(&sm.hv[8 + rs]); h2 = lds_ld(&sm.hv[16 + rs]);
      if (__all(h0 != SENT && h1 != SENT && h2 != SENT) || it >= SPIN_L) break;
      __builtin_amdgcn_s_sleep(1);
    }
#pragma unroll
    for (int e = 0; e < 4; ++e) {
      y[2 * e] = __uint_as_float(h0) * bflo(dw[0][e]) + __uint_as_float(h1) * bflo(dw[1][e]) + __uint_as_float(h2) * bflo(dw[2][e]);
      y[2 * e + 1] = __uint_as_float(h0) * bfhi(dw[0][e]) + __uint_as_float(h1) * bfhi(dw[1][e]) + __uint_as_float(h2) * bfhi(dw[2][e]);
    }
    if (w == 0) STAMP(13);
  }
  FENCE();
  if (!ow) {
    const int p0 = w - 9;
    u32 wb_;
    for (u32 it = 0;; ++it) {
      wb_ = lds_ld(&sm.wgt[(p0 + 7 * (l >> 4)) & 15]);
      if (__all(wb_ != SENT) || it >= SPIN_L) break;
      __builtin_amdgcn_s_sleep(1);
    }
    const unsigned char* op = ws + WS_OX + (u32)u * (16 * OXS) + (u32)p0 * OXS;
    u32 pend = p0 < 2 ? 7u : 3u;
    for (u32 it = 0; it < SPIN_G && pend; ++it) {
      u32x16 v[3];
      if (pend & 1u) asm volatile("s_load_dwordx16 %0, %1, 0x0 glc" : "=&s"(v[0]) : "s"(op) : "memory");
      if (pend & 2u) asm volatile("s_load_dwordx16 %0, %1, %2 glc" : "=&s"(v[1]) : "s"(op), "n"(7 * OXS) : "memory");
      if (pend & 4u) asm volatile("s_load_dwordx16 %0, %1, %2 glc" : "=&s"(v[2]) : "s"(op), "n"(14 * OXS) : "memory");
      asm volatile("s_waitcnt lgkmcnt(0)" ::: "memory");
#pragma unroll
      for (int k = 0; k < 3; ++k) {
        if (pend & (1u << k)) {
          asm volatile("" : "+s"(v[k]));
          u32 mx = v[k][0];
#pragma unroll
          for (int e = 1; e < 16; ++e) mx = mx > v[k][e] ? mx : v[k][e];
          if (mx != SENT) {
            pend &= ~(1u << k);
            u32 t = 0;
#pragma unroll
            for (int e = 0; e < 16; ++e) asm("v_writelane_b32 %0, %1, %2" : "+v"(t) : "s"(v[k][e]), "n"(16 * k + e));
            const u32 r = nosent(__uint_as_float(t) * __uint_as_float(wb_));
            const int p = p0 + 7 * k;
            if ((l >> 4) == k) lds_st(&sm.op[(((l >> 1) & 7) * 8 + (p >> 1)) * 4 + (l & 1) * 2 + (p & 1)], r);
          }
        }
      }
    }
    if (pend) { lds_st(&sm.op[4 * l], 0u); lds_st(&sm.op[4 * l + 1], 0u); lds_st(&sm.op[4 * l + 2], 0u); lds_st(&sm.op[4 * l + 3], 0u); }
    if (w == 9) STAMP(20);
    if (w == 13) STAMP(15);
  }
  if (ow) {
    float wf[16];
#pragma unroll
    for (int e = 0; e < 4; ++e) { wf[2 * e] = bflo(dw[3][e]); wf[2 * e + 1] = bfhi(dw[3][e]); wf[8 + 2 * e] = bflo(dw[4][e]); wf[9 + 2 * e] = bfhi(dw[4][e]); }
    if (w == 0) STAMP(21);
    u32x4 o4;
    for (u32 it = 0;; ++it) {
      o4 = lds_ld4(&sm.op[4 * l]);
      if (__all(o4[0] != SENT && o4[1] != SENT && o4[2] != SENT && o4[3] != SENT) || it >= SPIN_L) break;
    }
    float o0 = __uint_as_float(o4[0]) + __uint_as_float(o4[1]), o1 = __uint_as_float(o4[2]) + __uint_as_float(o4[3]);
    o0 += dpp<0xB1>(o0); o1 += dpp<0xB1>(o1);
    o0 += dpp<0x4E>(o0); o1 += dpp<0x4E>(o1);
    o0 += dpp<0x141>(o0); o1 += dpp<0x141>(o1);
#pragma unroll
    for (int e = 0; e < 8; ++e) y[e] += o0 * wf[e] + o1 * wf[8 + e];
    float a4[4], a2[2];
#pragma unroll
    for (int e = 0; e < 4; ++e) a4[e] = red32(y[e], y[e + 4]);
#pragma unroll
    for (int e = 0; e < 2; ++e) a2[e] = red16(a4[e], a4[e + 2]);
    const float t_ = (l & 8) ? a2[0] : a2[1], kp_ = (l & 8) ? a2[1] : a2[0];
    const float a1 = kp_ + dpp<0x128>(t_);
    __builtin_amdgcn_raw_buffer_store_b32(nosent(a1), rWS, WS_FX + (u32)(4 * w + ((l >> 1) & 3)) * 16384u + (u32)u * 64u +
                                          (8 * (l & 1) + 4 * (l >> 5) + 2 * ((l >> 4) & 1) + ((l >> 3) & 1)) * 4, 0, AUX_SC1);
    if (w == 0) STAMP(14);
  }
  if (fcons) {
    if (w >= 9) {
      for (u32 it = 0; it < SPIN_L; ++it) {
        const u32x4 o4 = lds_ld4(&sm.op[4 * l]);
        if (__all(o4[0] != SENT && o4[1] != SENT && o4[2] != SENT && o4[3] != SENT)) break;
        __builtin_amdgcn_s_sleep(2);
      }
    }
#define FDELAY 4
    __builtin_amdgcn_s_sleep(FDELAY);
    const int col = 4 * (l & 3) + 2 * (l >> 5) + ((l >> 4) & 1);
    const u32 xo = *((lds_u16*)sm.xs + cidx * 16 + col);
    u32 fa = WS_FX + cidx * 16384u + (u32)w * 1024u + l * 16;
    u32x4 t = {0u, 0u, 0u, 0u};
    for (u32 it = 0; it < SPIN_G; ++it) {
      asm volatile("" ::: "memory");
      const u32x4 r = __builtin_amdgcn_raw_buffer_load_b128(rWS, fa, 0, AUX_SC1);
      if (fa != OOR && r[0] != SENT && r[1] != SENT && r[2] != SENT && r[3] != SENT) { t = r; fa = OOR; }
      if (__all(fa == OOR)) break;
    }
#define FRED(t) ({ float c_ = red16(red32(__uint_as_float(t[0]), __uint_as_float(t[2])), red32(__uint_as_float(t[1]), __uint_as_float(t[3]))); \
      c_ += dpp<0x124>(c_); c_ + dpp<0x128>(c_); })
    {
      const float c = FRED(t);
      if ((l & 12) == 0) lds_st(&sm.fin[w][col], nosent(c));
    }
    if (w == 0) STAMP(22);
    u32x4 f0;
    for (u32 it = 0;; ++it) {
      f0 = lds_ld4(&sm.fin[0][4 * l]);
      if (__all(f0[0] != SENT && f0[1] != SENT && f0[2] != SENT && f0[3] != SENT) || it >= SPIN_L) break;
      __builtin_amdgcn_s_sleep(1);
    }
    const float c = FRED(f0);
    const __bf16 r = (__bf16)(bflo(xo) + c);
    if ((l & 12) == 0) out[cidx * 16 + col] = __builtin_bit_cast(unsigned short, r);
    if (cidx == 0 && l == 0) __builtin_amdgcn_raw_buffer_store_b32(sm.ev + 1u, rWS, WS_E, 0, AUX_SC1);
    if (w == 0) STAMP(23);
  }
}

extern "C" void kernel_launch(void* const* d_in, const int* in_sizes, int n_in, void* d_out, int out_size, void* d_ws, size_t ws_size,
                              hipStream_t stream) {
  fused_decode<<<256, 1024, 0, stream>>>((const unsigned short*)d_in[0], (const unsigned short*)d_in[1], (const unsigned short*)d_in[2],
                                         (const unsigned short*)d_in[3], (const unsigned short*)d_in[4], (const unsigned short*)d_in[6],
                                         (const unsigned short*)d_in[7], (const unsigned short*)d_in[8], (unsigned char*)d_ws,
                                         (unsigned short*)d_out);
}
```

```cpp
#include <hip/hip_runtime.h>
#include <hip/hip_bf16.h>
#include <cstdint>

typedef unsigned u32;
typedef u32 u32x4 __attribute__((ext_vector_type(4)));
typedef u32 u32x2 __attribute__((ext_vector_type(2)));
typedef u32 u32x16 __attribute__((ext_vector_type(16)));
typedef float f32x4 __attribute__((ext_vector_type(4)));
typedef float f32x2 __attribute__((ext_vector_type(2)));
typedef __bf16 bf16x2 __attribute__((ext_vector_type(2)));
typedef __bf16 bf16x8 __attribute__((ext_vector_type(8)));
typedef __amdgpu_buffer_rsrc_t rsrc_t;

#define WIN 3

constexpr u32 WS_E = 0;
constexpr u32 WS_QX = 4096;
constexpr u32 WS_HX = WS_QX + 256 * 512;
constexpr u32 WS_FL = WS_HX + 256 * 1024;
#define OXS 64
constexpr u32 WS_AG = WS_FL + 256 * 128;
constexpr u32 WS_ML = WS_AG + 16 * 512;
constexpr u32 WS_OX = WS_ML + 256 * 128;
constexpr u32 WS_FX = WS_OX + 256 * 16 * OXS;
constexpr u32 WS_END = WS_FX + 36 * 16384;
constexpr u32 SPIN_G = 40000;
constexpr u32 SPIN_L = 4000000;
constexpr u32 SENT = 0xFFFFFFFFu;
constexpr u32 OOR = 0xFFF00000u;

#define STAMP(i) do { } while (0)
#define WST(id) do { } while (0)

#define AUX_NT 2
#define AUX_SC1 16
#define FENCE() __builtin_amdgcn_sched_barrier(0)
#define THR(n) do { asm volatile("s_waitcnt vmcnt(%0)" :: "n"(n) : "memory"); FENCE(); } while (0)
#define THROTTLE() THR(WIN - 1)
#define LDSWAIT() asm volatile("s_waitcnt lgkmcnt(0)" ::: "memory")

__device__ __forceinline__ float bflo(u32 u) { return __uint_as_float(u << 16); }
__device__ __forceinline__ float bfhi(u32 u) { return __uint_as_float(u & 0xffff0000u); }
__device__ __forceinline__ float rbf(float a) { return bflo(__builtin_bit_cast(unsigned short, (__bf16)a)); }
template <int CTRL> __device__ __forceinline__ float dpp(float x) {
  return __int_as_float(__builtin_amdgcn_update_dpp(0, __float_as_int(x), CTRL, 0xf, 0xf, true));
}
template <int N> __device__ __forceinline__ float rorf(float x) {
  if constexpr ((N & 15) == 0) return x; else return dpp<0x120 + (N & 15)>(x);
}
template <int N> __device__ __forceinline__ u32 roru(u32 x) {
  if constexpr ((N & 15) == 0) return x; else return (u32)__builtin_amdgcn_update_dpp(0, (int)x, 0x120 + (N & 15), 0xf, 0xf, true);
}
__device__ __forceinline__ float red32(float a, float b) {
  auto r = __builtin_amdgcn_permlane32_swap(__float_as_uint(a), __float_as_uint(b), false, false);
  return __uint_as_float(r[0]) + __uint_as_float(r[1]);
}
__device__ __forceinline__ float red16(float a, float b) {
  auto r = __builtin_amdgcn_permlane16_swap(__float_as_uint(a), __float_as_uint(b), false, false);
  return __uint_as_float(r[0]) + __uint_as_float(r[1]);
}
__device__ __forceinline__ float max32(float a) {
  auto r = __builtin_amdgcn_permlane32_swap(__float_as_uint(a), __float_as_uint(a), false, false);
  return fmaxf(__uint_as_float(r[0]), __uint_as_float(r[1]));
}
__device__ __forceinline__ float max16(float a) {
  auto r = __builtin_amdgcn_permlane16_swap(__float_as_uint(a), __float_as_uint(a), false, false);
  return fmaxf(__uint_as_float(r[0]), __uint_as_float(r[1]));
}
__device__ __forceinline__ float rowsum16(float s) {
  s += dpp<0xB1>(s); s += dpp<0x4E>(s); s += dpp<0x141>(s); s += dpp<0x140>(s);
  return s;
}
__device__ __forceinline__ float rowmax16(float s) {
  s = fmaxf(s, dpp<0xB1>(s)); s = fmaxf(s, dpp<0x4E>(s)); s = fmaxf(s, dpp<0x141>(s)); s = fmaxf(s, dpp<0x140>(s));
  return s;
}
__device__ __forceinline__ float wavesum(float s) { s = rowsum16(s); s = red16(s, s); s = red32(s, s); return s; }
__device__ __forceinline__ rsrc_t mkr(const void* p, u32 n) { return __builtin_amdgcn_make_buffer_rsrc((void*)p, 0, n, 0x00020000); }
__device__ __forceinline__ u32x4 gran(float a, float b, u32 tag) { u32x4 r = {__float_as_uint(a), tag, __float_as_uint(b), tag}; return r; }
__device__ __forceinline__ bool tagok(u32x4 v, u32 tag) { return v[1] == tag && v[3] == tag; }
typedef __attribute__((address_space(3))) volatile u32 lds_u32;
typedef __attribute__((address_space(3))) volatile u32x4 lds_u32x4;
typedef __attribute__((address_space(3))) volatile unsigned short lds_u16;
__device__ __forceinline__ u32 lds_ld(u32* p) { return *(lds_u32*)p; }
__device__ __forceinline__ u32x4 lds_ld4(u32* p) { return *(lds_u32x4*)p; }
__device__ __forceinline__ void lds_st(u32* p, u32 v) { *(lds_u32*)p = v; }
#define PIN8(a) asm volatile("" : "+v"(a[0]), "+v"(a[1]), "+v"(a[2]), "+v"(a[3]), "+v"(a[4]), "+v"(a[5]), "+v"(a[6]), "+v"(a[7]))
__device__ __forceinline__ u32 nosent(float v) { const u32 t = __float_as_uint(v); return t == SENT ? 0x7FC00000u : t; }
__device__ __forceinline__ u32 arrive(u32* c, int l) {
  u32 old = 0;
  LDSWAIT();
  if (l == 0) old = __hip_atomic_fetch_add(c, 1u, __ATOMIC_RELAXED, __HIP_MEMORY_SCOPE_WORKGROUP);
  old = __builtin_amdgcn_readfirstlane(old);
  asm volatile("" ::: "memory");
  return old;
}
__device__ __forceinline__ f32x4 mfma(u32x4 a, u32x4 b, f32x4 c) {
  return __builtin_amdgcn_mfma_f32_16x16x32_bf16(__builtin_bit_cast(bf16x8, a), __builtin_bit_cast(bf16x8, b), c, 0, 0, 0);
}

struct __align__(16) Smem {
  float qp[8][64];
  float hs[12][64];
  float acc[8][256];
  float wmax[2][8];
  float wl[2][8];
  u32 qv[2][2][64];
  u32 xs[320];
  float xn[640];
  u32 op[256];
  u32 fin[16][16];
  u32 hv[24];
  u32 wgt[16];
  u32 cq, ch, cm, ca, fq, fx, cv, ff;
  u32 an, pad3, pad4, ev;
};
#define WA 3
#define NA 3
#define WB 6
#define NB 32
#define WC 6
#define NC 32
#define WD 6
#define BARI 2
#define TRIGN 8u
#define QSLP 4
#define HSLP 8
#define HSLP0 32
#define AUX_1ST 0
#define MSLP 4
#define MSLP0 16
#define AUX_E 0
#define WS_ 3
#define QPOS 7
#define TRIG 15
#define PACE_AT(base, T) do {                                                                                               \
      asm volatile("1: s_memrealtime s[94:95]\n s_waitcnt lgkmcnt(0)\n s_sub_u32 s94, s94, %0\n s_cmpk_lt_i32 s94, %1\n"    \
                   " s_cbranch_scc0 2f\n s_sleep 1\n s_branch 1b\n2:"                                                      \
                   :: "s"(base), "n"(T) : "s94", "s95", "scc", "memory");                                                   \
      FENCE(); } while (0)
#define PN0 3
#define PT0 2150
#define N1 4
#define N2 17
#define TAU1 285
#define TAU2 400
#define TAU3 285
constexpr int TK(int n) { return n <= N1 ? PT0 + (n - PN0) * TAU1 : n <= N2 ? TK(N1) + (n - N1) * TAU2 : TK(N2) + (n - N2) * TAU3; }
#define GT0 2600
#define GTAU 800
#define DOFF 300
#define DTAU 400
#define DSTAG 0
#define AOFF 0
#define ACL 100
#define ANCHOR(dst) do { const u32 a_ = lds_ld(&sm.an);                                                                     \
      dst = __builtin_amdgcn_readfirstlane(a_ ? tb + (u32)min(max((int)(a_ - tb), -ACL), ACL) : tb); FENCE(); } while (0)
#define BUMP(c) do { if (l == 0) __hip_atomic_fetch_add(&sm.c, 1u, __ATOMIC_RELAXED, __HIP_MEMORY_SCOPE_WORKGROUP); } while (0)
#define LWAIT(c, n) do { for (u32 it = 0; lds_ld(&sm.c) != (n) && it < SPIN_L; ++it) __builtin_amdgcn_s_sleep(1); asm volatile("" ::: "memory"); } while (0)

__global__ void __launch_bounds__(1024, 4)
fused_decode(const unsigned short* __restrict__ x, const unsigned short* __restrict__ Kc, const unsigned short* __restrict__ Vc,
             const unsigned short* __restrict__ wq, const unsigned short* __restrict__ wo, const unsigned short* __restrict__ wg,
             const unsigned short* __restrict__ wu, const unsigned short* __restrict__ wd, unsigned char* __restrict__ ws,
             unsigned short* __restrict__ out) {
  __shared__ Smem sm;
  const int b = blockIdx.x;
  const int w = __builtin_amdgcn_readfirstlane(threadIdx.x >> 6);
  const int l = threadIdx.x & 63;
#define PXA 0
#define PXB 0
  const int u = ((b + PXA) & 7) * 32 + ((b >> 3) ^ PXB);
  const int g = u >> 4, j = u & 15, k4 = u & 3;
  const bool fcons = b < 36;
  const u32 cidx = (u32)b;
  const u32 tb = (u32)__builtin_amdgcn_s_memrealtime();
  FENCE();
  asm volatile("" :: "s"(x), "s"(Kc), "s"(Vc), "s"(wq), "s"(wo), "s"(wg), "s"(wu), "s"(wd), "s"(ws));
  const rsrc_t rWS = mkr(ws, WS_END);
  const bool ow = w < 9;
  const rsrc_t rD = mkr(wd, 4608u * 1152u), rO = mkr(wo, 4096u * 1152u);
  const u32 co = (u32)w * 128u + (l & 7) * 16;
  const u32 od = ow ? (u32)(18 * u + (l >> 3)) * 1152u + co : OOR;
  const u32 oo = ow ? (u32)(16 * u + (l >> 3)) * 1152u + co : OOR;
  u32x4 dw[5];
#define DWLOADS() do {                                                                                     \
    dw[0] = __builtin_amdgcn_raw_buffer_load_b128(rD, od, 0, AUX_NT);                                      \
    dw[1] = __builtin_amdgcn_raw_buffer_load_b128(rD, od, 8 * 1152, AUX_NT);                               \
    dw[2] = __builtin_amdgcn_raw_buffer_load_b128(rD, l < 16 ? od : OOR, 16 * 1152, AUX_NT);               \
    dw[3] = __builtin_amdgcn_raw_buffer_load_b128(rO, oo, 0, AUX_NT);                                      \
    dw[4] = __builtin_amdgcn_raw_buffer_load_b128(rO, oo, 8 * 1152, AUX_NT); FENCE(); } while (0)

  if (w < 8) {
    const u32 ts0 = tb + (u32)(w * (GTAU / 60));
    const rsrc_t rX = mkr(x, 1152), rQ = mkr(wq, 576u * 8192u), rG = mkr((g & 1) ? wu : wg, 576u * 9216u);
    const int rb = 144 * k4 + 18 * w;
    const u32 qo = (u32)(rb + (l >> 3)) * 8192u + (u32)(256 * g + 64 * (j >> 2)) * 2 + (l & 7) * 16;
    const int uB = 9 * j + 3 * (w >> 1) + 1, uA = uB - 1 + 2 * (w & 1);
    const int rA = 36 * (uA & 15), rB = 36 * (uB & 15) + 18 * (w & 1);
    const u32 gv = w < 6 ? (u32)(l >> 3) * 9216u + (u32)(u >> 5) * 1152u + (l & 7) * 16 : OOR;
    const u32 gB = w < 6 ? (u32)(rB * 9216 + (uB >> 4) * 128) : 0u, gA = (u32)(rA * 9216 + (uA >> 4) * 128);
#define AUX_G AUX_NT
    u32x4 xa = {0u, 0u, 0u, 0u};
    u32 xb = 0u, Ev = 0u;
    if (w == 0) {
      xa = __builtin_amdgcn_raw_buffer_load_b128(rX, l * 16, 0, 0);
      xb = __builtin_amdgcn_raw_buffer_load_b32(rX, 1024 + l * 4, 0, 0);
      Ev = __builtin_amdgcn_raw_buffer_load_b32(rWS, WS_E, 0, AUX_E);
    }
    {
      const u32x4 sv = {SENT, SENT, SENT, SENT};
      if (w == 7) __builtin_amdgcn_raw_buffer_store_b128(sv, rWS, WS_OX + (u32)u * (16 * OXS) + (l >> 2) * OXS + (l & 3) * 16, 0, AUX_SC1);
      if (w == 6) __builtin_amdgcn_raw_buffer_store_b128(sv, rWS, l < 8 ? WS_ML + (u32)u * 128u + l * 16 : OOR, 0, AUX_SC1);
    }
    FENCE();
    u32x4 fq_[3], fg[7];
    fq_[0] = __builtin_amdgcn_raw_buffer_load_b128(rQ, qo, 0, AUX_NT); FENCE();
    fq_[1] = __builtin_amdgcn_raw_buffer_load_b128(rQ, qo, 8 * 8192, AUX_NT); FENCE();
    fq_[2] = __builtin_amdgcn_raw_buffer_load_b128(rQ, l < 16 ? qo : OOR, 16 * 8192, AUX_NT); FENCE();
    if (fcons) {
      const u32x4 sv = {SENT, SENT, SENT, SENT};
#pragma unroll
      for (int i = 0; i < 2; ++i) __builtin_amdgcn_raw_buffer_store_b128(sv, rWS, WS_FX + cidx * 16384u + (u32)w * 2048u + l * 16, i * 1024, AUX_SC1);
    }
    FENCE();
    if (threadIdx.x < 281) {
      const u32 iv = threadIdx.x < 276 ? SENT : 0u;
      *(u32x2*)&sm.op[2 * threadIdx.x] = u32x2{iv, iv};
    }
    LDSWAIT(); __builtin_amdgcn_s_barrier(); FENCE(); WST(0);
    fg[0] = __builtin_amdgcn_raw_buffer_load_b128(rG, gv, gB, AUX_G); FENCE();
    if (w == 0) {
      float xf[10];
#pragma unroll
      for (int e = 0; e < 4; ++e) { xf[2 * e] = bflo(xa[e]); xf[2 * e + 1] = bfhi(xa[e]); }
      xf[8] = bflo(xb); xf[9] = bfhi(xb);
      const float mean = wavesum((((xf[0] + xf[1]) + (xf[2] + xf[3])) + ((xf[4] + xf[5]) + (xf[6] + xf[7]))) + (xf[8] + xf[9])) * (1.0f / 576.0f);
      const float mb = rbf(mean);
      float xd[10];
#pragma unroll
      for (int e = 0; e < 10; ++e) { xd[e] = rbf(xf[e] - mb); xf[e] -= mean; }
      float v0 = xf[0] * xf[0], v1 = xf[1] * xf[1];
#pragma unroll
      for (int e = 2; e < 8; e += 2) { v0 += xf[e] * xf[e]; v1 += xf[e + 1] * xf[e + 1]; }
      v0 += l < 32 ? xf[8] * xf[8] + xf[9] * xf[9] : 0.f;
      const float rs = __builtin_amdgcn_rsqf(wavesum(v0 + v1) * (1.0f / 576.0f) + 1e-5f);
#pragma unroll
      for (int e = 0; e < 10; ++e) xf[e] = xf[e] * rs;
      *(f32x4*)&sm.xn[8 * l] = f32x4{xf[0], xf[1], xf[2], xf[3]};
      *(f32x4*)&sm.xn[8 * l + 4] = f32x4{xf[4], xf[5], xf[6], xf[7]};
      *(f32x2*)&sm.xn[512 + 2 * l] = f32x2{xf[8], xf[9]};
      *(u32x4*)&sm.xs[4 * l] = xa;
      sm.xs[256 + l] = xb;
      if (l == 0) sm.ev = Ev;
      LDSWAIT();
      if (l == 0) lds_st(&sm.fx, 1u);
      STAMP(2);
    }
    FENCE();
    LWAIT(fx, 1u);
    const u32 tag = __builtin_amdgcn_readfirstlane(sm.ev) * 0x9E3779B1u + 0x7F4A7C15u;
    float xq[3], xg[8];
#pragma unroll
    for (int i = 0; i < 3; ++i) xq[i] = sm.xn[rb + (l >> 3) + 8 * i];
    asm volatile("" : "+v"(xq[0]), "+v"(xq[1]), "+v"(xq[2]));
    FENCE();
    THR(1);
    WST(1);
#define FACC(N, G, F, X) do {                                                                                          \
      _Pragma("unroll") for (int e = 0; e < 4; ++e) G[e] = f32x2{X[0], X[0]} * f32x2{bflo(F[0][e]), bfhi(F[0][e])};    \
      _Pragma("unroll") for (int i = 1; i < N; ++i)                                                                    \
        _Pragma("unroll") for (int e = 0; e < 4; ++e) G[e] += f32x2{X[i], X[i]} * f32x2{bflo(F[i][e]), bfhi(F[i][e])}; \
    } while (0)
    {
      f32x2 g2[4];
      FACC(3, g2, fq_, xq);
      float a4[4], a2[2];
#pragma unroll
      for (int e = 0; e < 4; ++e) a4[e] = red32(g2[e >> 1][e & 1], g2[2 + (e >> 1)][e & 1]);
#pragma unroll
      for (int e = 0; e < 2; ++e) a2[e] = red16(a4[e], a4[e + 2]);
      const float t_ = (l & 8) ? a2[0] : a2[1], kp_ = (l & 8) ? a2[1] : a2[0];
      sm.qp[w][8 * (l & 7) + 4 * (l >> 5) + 2 * ((l >> 4) & 1) + ((l >> 3) & 1)] = kp_ + dpp<0x128>(t_);
      if (w == 0) STAMP(1);
      if (arrive(&sm.cq, l) == 7u) {
        const int P_ = l & 31, hf = l >> 5;
        const f32x2 s_ = (*(const f32x2*)&sm.qp[hf * 4][2 * P_] + *(const f32x2*)&sm.qp[hf * 4 + 1][2 * P_]) +
                         (*(const f32x2*)&sm.qp[hf * 4 + 2][2 * P_] + *(const f32x2*)&sm.qp[hf * 4 + 3][2 * P_]);
        const float s0 = red32(s_[0], s_[0]), s1 = red32(s_[1], s_[1]);
        if (l < 32) __builtin_amdgcn_raw_buffer_store_b128(gran(s0, s1, tag), rWS, WS_QX + (u32)u * 512u + P_ * 16, 0, AUX_SC1);
        __builtin_amdgcn_raw_buffer_store_b32(tag, rWS, l < 16 ? WS_FL + (u32)(g * 16 + l) * 128u + j * 4 : OOR, 0, AUX_SC1);
        if (fcons) __builtin_amdgcn_raw_buffer_store_b64(u32x2{tb, tag}, rWS, l < 16 ? WS_AG + (u32)l * 512u + cidx * 8u : OOR, 0, AUX_SC1);
        STAMP(3);
      }
      FENCE();
    }
    if (w < 6) {
      const float* const xr = &sm.xn[l >> 3];
      xg[0] = xr[rB]; xg[1] = xr[rB + 8];
      xg[2] = xr[l < 16 ? rB + 16 : rA - 2];
      xg[4] = xr[rA + 6]; xg[5] = xr[rA + 14]; xg[6] = xr[rA + 22]; xg[7] = xr[rA + 30];
      xg[7] = l < 48 ? xg[7] : 0.f;
      xg[3] = l < 16 ? 0.f : xg[2];
      xg[2] = l < 16 ? xg[2] : 0.f;
      const int c0 = (9 * j) >> 4, n0 = 16 - ((9 * j) & 15);
      const int s0 = j - ((c0 * 455) >> 8);
      const int c1 = n0 < 9 ? c0 + 1 : j == 0 ? 0 : j == 6 ? 4 : 8, s1 = n0 < 9 ? 0 : 2;
      const u32 cA = (u32)(64 * c0 + l), cB = (u32)(64 * c1 + l), qA = (cA * 3641u) >> 16, qB = (cB * 3641u) >> 16;
      const u32 hbase = WS_HX + (u32)(u & ~31) * 1024u + (u32)(g & 1) * 432u;
      u32 hA = hbase + (u32)s0 * 144u + qA * 1024u + (cA - 18u * qA) * 8u;
      u32 hB = (n0 < 9 || j == 0 || j == 6 || j == 15) ? hbase + (u32)s1 * 144u + qB * 1024u + (cB - 18u * qB) * 8u : OOR;
      u32 gv2 = gv + (l < 16 ? gB + 16u * 9216u : gA - 2u * 9216u), gv6 = l < 48 ? gv : OOR;
      PIN8(xg);
      asm volatile("" : "+v"(hA), "+v"(hB), "+v"(gv2), "+v"(gv6));
      const int cl = 8 * (l & 7) + 4 * (l >> 5) + 2 * ((l >> 4) & 1) + ((l >> 3) & 1);
#define GLD(k, vo, so) do { PACE_AT(ts0, (GT0 + ((k) - 1) * GTAU) / 10); fg[k] = __builtin_amdgcn_raw_buffer_load_b128(rG, vo, so, AUX_G); FENCE(); } while (0)
#define COLSUM(G) ({ float a4_[4], a2_[2];                                                                              \
      _Pragma("unroll") for (int e = 0; e < 4; ++e) a4_[e] = red32(G[e >> 1][e & 1], G[2 + (e >> 1)][e & 1]);           \
      _Pragma("unroll") for (int e = 0; e < 2; ++e) a2_[e] = red16(a4_[e], a4_[e + 2]);                                 \
      const float t_ = (l & 8) ? a2_[0] : a2_[1], kp_ = (l & 8) ? a2_[1] : a2_[0];                                      \
      kp_ + dpp<0x128>(t_); })
      GLD(1, gv, gB + 8u * 9216u); GLD(2, gv2, 0); GLD(3, gv, gA + 6u * 9216u); GLD(4, gv, gA + 14u * 9216u);
      {
        f32x2 g2[4];
        FACC(3, g2, fg, xg);
        sm.hs[2 * (w & 6) + 1 + (w & 1)][cl] = COLSUM(g2);
        FENCE();
      }
      GLD(5, gv, gA + 22u * 9216u); GLD(6, gv6, gA + 30u * 9216u);
      {
        f32x2 g2[4];
        FACC(5, g2, (fg + 2), (xg + 3));
        sm.hs[2 * (w & 6) + 3 * (w & 1)][cl] = COLSUM(g2);
        WST(2);
        if (arrive(&sm.ch, l) == 5u) {
          float t[12], a0 = 0.f, a1 = 0.f;
#pragma unroll
          for (int i = 0; i < 12; ++i) t[i] = sm.hs[i][l];
#pragma unroll
          for (int i = 0; i < 12; ++i) {
            const bool first = 3 * (i >> 2) + ((i & 3) + 1) / 2 < n0;
            a0 += first ? t[i] : 0.f; a1 += first ? 0.f : t[i];
          }
          const u32x2 r0 = {__float_as_uint(a0), tag}, r1 = {__float_as_uint(a1), tag};
          __builtin_amdgcn_raw_buffer_store_b64(r0, rWS, hA, 0, AUX_SC1);
          __builtin_amdgcn_raw_buffer_store_b64(r1, rWS, hB, 0, AUX_SC1);
          STAMP(6);
        }
        FENCE();
      }
    } else {
      if (w == 6) {
        const unsigned char* fp = ws + WS_FL + (u32)u * 128u;
        LWAIT(cq, 8u);
        for (u32 it = 0; it < SPIN_G; ++it) {
          u32x16 v;
          __builtin_amdgcn_s_sleep(QSLP);
          asm volatile("s_load_dwordx16 %0, %1, 0x0 glc\n s_waitcnt lgkmcnt(0)" : "=&s"(v) : "s"(fp) : "memory");
          u32 bad = 0;
#pragma unroll
          for (int e = 0; e < 16; ++e) bad |= v[e] ^ tag;
          if (bad == 0) break;
        }
        if (l == 0) lds_st(&sm.ff, 1u);
      } else {
        LWAIT(ff, 1u);
      }
      if (w == 6) STAMP(4);
      const u32 g1a = WS_QX + (u32)(g * 16 + (w - 6) * 8 + (l >> 4)) * 512u + (l & 15) * 16;
      u32x4 g1[4];
#pragma unroll
      for (int i = 0; i < 4; ++i) g1[i] = __builtin_amdgcn_raw_buffer_load_b128(rWS, g1a + (i & 1) * 256 + (i >> 1) * 2048, 0, AUX_1ST);
      asm volatile("" : "+v"(g1[0]), "+v"(g1[1]), "+v"(g1[2]), "+v"(g1[3]));
      if (w == 6) STAMP(16);
      if (!__all(tagok(g1[0], tag) && tagok(g1[1], tag) && tagok(g1[2], tag) && tagok(g1[3], tag))) {
        for (u32 it = 0;; ++it) {
          asm volatile("" ::: "memory");
#pragma unroll
          for (int i = 0; i < 4; ++i) g1[i] = __builtin_amdgcn_raw_buffer_load_b128(rWS, g1a + (i & 1) * 256 + (i >> 1) * 2048, 0, AUX_SC1);
          asm volatile("" : "+v"(g1[0]), "+v"(g1[1]), "+v"(g1[2]), "+v"(g1[3]));
          if (__all(tagok(g1[0], tag) && tagok(g1[1], tag) && tagok(g1[2], tag) && tagok(g1[3], tag)) || it >= SPIN_G) break;
        }
      }
#pragma unroll
      for (int i = 0; i < 4; ++i) {
        float a = red16(__uint_as_float(g1[i][0]), __uint_as_float(g1[i][2]));
        a = red32(a, a);
        const u32 hib = __builtin_bit_cast(unsigned short, (__bf16)a);
        *((lds_u16*)&sm.qv[w - 6][l >> 5][16 * i + (l & 15)] + ((l >> 4) & 1)) = (unsigned short)(l < 32 ? hib : 0u);
      }
      LDSWAIT();
      BUMP(fq);
      if (w == 6) STAMP(5);
      WST(3);
    }
    FENCE();
    if (w == 7) {
      {
        const u32 ab = l < 18 ? WS_AG + (u32)g * 512u + l * 16 : OOR;
        u32x4 e0;
        for (u32 it = 0;; ++it) {
          asm volatile("" ::: "memory");
          e0 = __builtin_amdgcn_raw_buffer_load_b128(rWS, ab, 0, AUX_SC1);
          if (__all(l >= 18 || tagok(e0, tag)) || it >= SPIN_G) break;
          __builtin_amdgcn_s_sleep(8);
        }
        const int d0 = min(max((int)(e0[0] - tb), -200), 200), d1 = min(max((int)(e0[2] - tb), -200), 200);
        const float dm = rowsum16((float)(d0 + d1)) * (1.0f / 32.0f);
        if (l == 0) lds_st(&sm.an, (tb + (u32)((int)(dm + 256.5f) - 256 + AOFF)) | 1u);
        STAMP(19);
      }
      const bool on = (l & 15) < 9 && l < 48;
      const u32 hb = on ? WS_HX + (u32)u * 1024u + (u32)(l >> 4) * 144u + (l & 15) * 16 : OOR;
      const unsigned char* hs = ws + WS_HX + (u32)u * 1024u;
      LWAIT(ch, 6u);
      for (u32 it = 0; it < SPIN_G; ++it) {
        u32 a0, a6; u32x4 a1, a2, a3, a4, a5;
        __builtin_amdgcn_s_sleep(HSLP);
        asm volatile("s_load_dword %0, %7, 0x4 glc\n s_load_dwordx4 %1, %7, 0x8c glc\n s_load_dwordx4 %2, %7, 0x11c glc\n s_load_dwordx4 %3, %7, 0x1ac glc\n"
                     " s_load_dwordx4 %4, %7, 0x23c glc\n s_load_dwordx4 %5, %7, 0x2cc glc\n s_load_dword %6, %7, 0x35c glc\n s_waitcnt lgkmcnt(0)"
                     : "=&s"(a0), "=&s"(a1), "=&s"(a2), "=&s"(a3), "=&s"(a4), "=&s"(a5), "=&s"(a6) : "s"(hs) : "memory");
        if (((a0 ^ tag) | (a6 ^ tag) | (a1[0] ^ tag) | (a1[2] ^ tag) | (a2[0] ^ tag) | (a2[2] ^ tag) | (a3[0] ^ tag) | (a3[2] ^ tag) |
             (a4[0] ^ tag) | (a4[2] ^ tag) | (a5[0] ^ tag) | (a5[2] ^ tag)) == 0) break;
      }
      u32x4 t0, t1;
      for (u32 it = 0;; ++it) {
        asm volatile("" ::: "memory");
        t0 = __builtin_amdgcn_raw_buffer_load_b128(rWS, hb, 0, AUX_SC1);
        t1 = __builtin_amdgcn_raw_buffer_load_b128(rWS, hb + 432, 0, AUX_SC1);
        if (__all(!on || (tagok(t0, tag) && tagok(t1, tag))) || it >= SPIN_G) break;
        __builtin_amdgcn_s_sleep(8);
      }
      float gs = red16(__uint_as_float(t0[0]), __uint_as_float(t0[2])), us = red16(__uint_as_float(t1[0]), __uint_as_float(t1[2]));
      gs = red32(gs, gs); us = red32(us, us);
      const float hval = on ? gs / (1.0f + __expf(-gs)) * us : 0.f;
      if (l < 32 && (l & 15) < 12) lds_st(&sm.hv[2 * (l & 15) + (l >> 4)], nosent(hval));
      STAMP(10);
    } else if (w == 6) {
      LWAIT(cm, 8u);
      const int h = (l >> 3) & 1;
      const float mi = sm.wmax[h][l & 7], li = sm.wl[h][l & 7];
      float M = fmaxf(mi, dpp<0xB1>(mi)); M = fmaxf(M, dpp<0x4E>(M)); M = fmaxf(M, dpp<0x141>(M));
      float L = li * __builtin_amdgcn_exp2f(mi - M);
      L += dpp<0xB1>(L); L += dpp<0x4E>(L); L += dpp<0x141>(L);
      const u32x2 ml = {nosent(M), nosent(L)};
      __builtin_amdgcn_raw_buffer_store_b64(ml, rWS, l < 16 ? WS_ML + (u32)(g * 16 + l) * 128u + j * 8 : OOR, 0, AUX_SC1);
      STAMP(17);
      __builtin_amdgcn_s_sleep(MSLP0);
      const unsigned char* mp = ws + WS_ML + (u32)u * 128u;
      u32x16 v[2];
      for (u32 it = 0; it < SPIN_G; ++it) {
        asm volatile("s_load_dwordx16 %0, %1, 0x0 glc" : "=&s"(v[0]) : "s"(mp) : "memory");
        asm volatile("s_load_dwordx16 %0, %1, 0x40 glc" : "=&s"(v[1]) : "s"(mp) : "memory");
        asm volatile("s_waitcnt lgkmcnt(0)" ::: "memory");
        u32 mx = 0;
#pragma unroll
        for (int k = 0; k < 2; ++k) {
          asm volatile("" : "+s"(v[k]));
#pragma unroll
          for (int e = 0; e < 16; ++e) mx = mx > v[k][e] ? mx : v[k][e];
        }
        if (mx != SENT) break;
        __builtin_amdgcn_s_sleep(MSLP);
      }
      u32 tm = 0, tl = 0;
#pragma unroll
      for (int k = 0; k < 2; ++k)
#pragma unroll
        for (int e = 0; e < 8; ++e) {
          asm("v_writelane_b32 %0, %1, %2" : "+v"(tm) : "s"(v[k][2 * e]), "n"(8 * k + e));
          asm("v_writelane_b32 %0, %1, %2" : "+v"(tl) : "s"(v[k][2 * e + 1]), "n"(8 * k + e));
        }
      asm volatile("s_nop 1" : "+v"(tm), "+v"(tl));
      const float mi2 = __uint_as_float(tm);
      const float e = __builtin_amdgcn_exp2f(mi2 - rowmax16(mi2));
      const float L2 = rowsum16(e * __uint_as_float(tl));
      if (l < 16) lds_st(&sm.wgt[l], nosent(e / L2));
      STAMP(18);
    }
    FENCE();
    for (u32 it = 0; lds_ld(&sm.cv) < TRIGN && it < SPIN_L; ++it) __builtin_amdgcn_s_sleep(1);
    asm volatile("" ::: "memory");
    if (w == 0) STAMP(24);
    u32 td0;
    ANCHOR(td0);
    td0 += (u32)(w * (DSTAG / 10));
#define DPACE(k) PACE_AT(td0, (TK(31) + DOFF + (k) * DTAU) / 10)
    DPACE(0); dw[0] = __builtin_amdgcn_raw_buffer_load_b128(rD, od, 0, AUX_NT); FENCE();
    DPACE(1); dw[1] = __builtin_amdgcn_raw_buffer_load_b128(rD, od, 8 * 1152, AUX_NT); FENCE();
    DPACE(2); dw[3] = __builtin_amdgcn_raw_buffer_load_b128(rO, oo, 0, AUX_NT); FENCE();
    DPACE(3); dw[4] = __builtin_amdgcn_raw_buffer_load_b128(rO, oo, 8 * 1152, AUX_NT); FENCE();
    dw[2] = __builtin_amdgcn_raw_buffer_load_b128(rD, l < 16 ? od : OOR, 16 * 1152, AUX_NT); FENCE();
    if (w == 0) STAMP(7);
    FENCE();
  } else {
    const rsrc_t rK = mkr(Kc, 33554432u), rV = mkr(Vc, 33554432u);
#define PKV 0
#define KVI 0
    const u32 kvb = (u32)(u ^ PKV) * 131072u + (u32)(w - 8) * 16384u;
#define TVO(i) (((i) & 3) * 1024)
#define TSO(i) (((i) >> 2) * 4096)
    const u32 voff = kvb + (u32)l * 16u;
    const u32 koff = kvb + (u32)(((l >> 2) & 3) * 256 + (l >> 4) * 64 + (l & 3) * 16);
    u32x4 kk[16], vv[16];
#define WINOF(n) ((n) < NA ? WA : (n) < NB ? WB : (n) < NC ? WC : WD)
    u32 tk = tb;
#define PACE(n) do { if ((n) >= PN0) PACE_AT(tk, TK(n) / 10); } while (0)
#define KLD(i) do { if ((i) >= WA) THR(WINOF(i) - 1);                                                                      \
      PACE(i);                                                                                                              \
      kk[i] = __builtin_amdgcn_raw_buffer_load_b128(rK, koff + TVO(i), TSO(i), AUX_NT); FENCE();                            \
      if ((i) == BARI) { __builtin_amdgcn_s_barrier(); FENCE(); WST(0); } } while (0)
#define VLD(i) do { if ((i) % 3 == 1 && (i) < 12) ANCHOR(tk); THR(WINOF(16 + (i)) - 1);                                                                               \
      PACE(16 + (i));                                                                                                       \
      vv[i] = __builtin_amdgcn_raw_buffer_load_b128(rV, voff + TVO(i), TSO(i), AUX_NT); FENCE();                            \
      if ((i) == TRIG) { BUMP(cv); FENCE(); } } while (0)
    KLD(0); KLD(1); KLD(2); KLD(3); KLD(4); KLD(5); KLD(6); KLD(7);
    WST(1);
    KLD(8); KLD(9); KLD(10); KLD(11); KLD(12); KLD(13); KLD(14); KLD(15);
    WST(2);
    VLD(0); VLD(1); VLD(2); VLD(3); VLD(4); VLD(5);
    if (QPOS >= 6) VLD(6);
    if (QPOS >= 7) VLD(7);
    WST(3);
    LWAIT(fq, 2u);
    if (w == 8) STAMP(8);
    WST(4);
    const int bank = (l >> 2) & 3;
    u32 pk[4];
    float mw;
    {
      const u32x4 qb = *(const u32x4*)&sm.qv[(l >> 1) & 1][l & 1][4 * ((l >> 4) * 4 + bank)];
      float sc[4];
#pragma unroll
      for (int ab = 0; ab < 4; ++ab) {
        const f32x4 z = {0.f, 0.f, 0.f, 0.f};
        const f32x4 d0 = mfma(kk[4 * ab], qb, z), d1 = mfma(kk[4 * ab + 1], qb, z), d2 = mfma(kk[4 * ab + 2], qb, z), d3 = mfma(kk[4 * ab + 3], qb, z);
        float a0 = ((d0[0] + rorf<-4>(d0[1])) + rorf<-8>(d0[2])) + rorf<-12>(d0[3]);
        float a1 = ((d1[1] + rorf<4>(d1[0])) + rorf<-4>(d1[2])) + rorf<-8>(d1[3]);
        float a2 = ((d2[2] + rorf<8>(d2[0])) + rorf<4>(d2[1])) + rorf<-4>(d2[3]);
        float a3 = ((d3[3] + rorf<12>(d3[0])) + rorf<8>(d3[1])) + rorf<4>(d3[2]);
        asm volatile("" : "+v"(a0), "+v"(a1), "+v"(a2), "+v"(a3));
        const float t01 = (l & 4) ? a1 : a0, t23 = (l & 4) ? a3 : a2;
        const float t = (l & 8) ? t23 : t01;
        sc[ab] = rbf(rbf(t + dpp<0xB1>(t)) * 0.08838834764831845f) * 1.4426950408889634f;
        asm volatile("" : "+v"(sc[ab]));
        if (ab == 1) { FENCE(); VLD(QPOS + 1); }
      }
      FENCE(); VLD(QPOS + 2);
      float m = fmaxf(fmaxf(sc[0], sc[1]), fmaxf(sc[2], sc[3]));
      m = fmaxf(m, rorf<4>(m)); m = fmaxf(m, rorf<8>(m));
      m = max32(max16(m));
      mw = m;
#pragma unroll
      for (int ab = 0; ab < 4; ++ab) {
        const float p = __builtin_amdgcn_exp2f(sc[ab] - m);
        const bf16x2 t = {(__bf16)p, (__bf16)p};
        pk[ab] = __builtin_bit_cast(u32, t);
      }
      float lt = (bfhi(pk[0]) + bfhi(pk[1])) + (bfhi(pk[2]) + bfhi(pk[3]));
      lt += rorf<4>(lt); lt += rorf<8>(lt);
      lt = red16(lt, lt); lt = red32(lt, lt);
      if ((l & 61) == 0) { sm.wmax[l >> 1][w - 8] = mw; sm.wl[l >> 1][w - 8] = lt; }
      LDSWAIT();
      BUMP(cm);
    }
    if (w == 8) STAMP(9);
    WST(5);
    FENCE();
    const u32 hm = (l & 1) ? 0xffff0000u : 0x0000ffffu;
    const u32 m0 = bank == 0 ? hm : 0u, m1 = bank == 1 ? hm : 0u, m2 = bank == 2 ? hm : 0u, m3 = bank == 3 ? hm : 0u;
    u32x4 pb[16];
#define PBSET(t) do { pb[t][0] = roru<4 * (0 - ((t) & 3))>(pk[(t) >> 2]) & m0; pb[t][1] = roru<4 * (1 - ((t) & 3))>(pk[(t) >> 2]) & m1;   \
                      pb[t][2] = roru<4 * (2 - ((t) & 3))>(pk[(t) >> 2]) & m2; pb[t][3] = roru<4 * (3 - ((t) & 3))>(pk[(t) >> 2]) & m3;   \
                      asm volatile("" : "+v"(pb[t])); } while (0)
    f32x4 acc = {0.f, 0.f, 0.f, 0.f};
#define PV(i) do { acc = mfma(vv[i], pb[i], acc); asm volatile("" : "+v"(acc)); FENCE(); } while (0)
    if (QPOS < 6) VLD(8);
    if (QPOS < 7) VLD(9);
    PBSET(0); PBSET(1); PBSET(2); PBSET(3); PV(0); PV(1); PV(2); PV(3);
    VLD(10);
    PBSET(4); PBSET(5); PBSET(6); PBSET(7); PV(4);
    VLD(11);
    PV(5); PBSET(8); PBSET(9); PBSET(10); PBSET(11); FENCE();
    VLD(12);
    PV(6); PBSET(12); PBSET(13); PBSET(14); PBSET(15); FENCE();
    VLD(13);
    PV(7);
    VLD(14);
    PV(8);
    VLD(15);
    PV(9);
    float fsc;
    {
      LWAIT(cm, 8u);
      const float mi = sm.wmax[(l >> 1) & 1][(l >> 2) & 7];
      float M = fmaxf(mi, rorf<4>(mi)); M = fmaxf(M, rorf<8>(M)); M = max16(M);
      fsc = __builtin_amdgcn_exp2f(mw - M);
      asm volatile("" : "+v"(fsc));
      FENCE();
    }
    PV(10); PV(11); PV(12); PV(13); PV(14); PV(15);
    {
      float* ap = &sm.acc[w - 8][(((l >> 1) & 1) * 8 + 2 * (l >> 4)) * 16 + (bank * 2 + (l & 1)) * 2];
      *(f32x2*)ap = f32x2{acc[0] * fsc, acc[1] * fsc};
      *(f32x2*)(ap + 16) = f32x2{acc[2] * fsc, acc[3] * fsc};
      if (w == 8) STAMP(11);
      if (w == 15) STAMP(25);
      WST(6);
      if (arrive(&sm.ca, l) == 7u) {
        f32x4 t[8];
#pragma unroll
        for (int i = 0; i < 8; ++i) t[i] = *(const f32x4*)&sm.acc[i][4 * l];
        const f32x4 t0 = ((t[0] + t[1]) + (t[2] + t[3])) + ((t[4] + t[5]) + (t[6] + t[7]));
        const u32x4 r = {nosent(t0[0]), nosent(t0[1]), nosent(t0[2]), nosent(t0[3])};
        __builtin_amdgcn_raw_buffer_store_b128(r, rWS, WS_OX + (u32)(g * 16 + (l >> 2)) * (16 * OXS) + j * OXS + (l & 3) * 16, 0, AUX_SC1);
        STAMP(12);
      }
      FENCE();
    }
  }
  FENCE();
  if (w == 8) DWLOADS();
  FENCE();
  float y[8];
  if (ow) {
    const int rs = l >> 3;
    u32 h0, h1, h2;
    for (u32 it = 0;; ++it) {
      h0 = lds_ld(&sm.hv[rs]); h1 = lds_ld(&sm.hv[8 + rs]); h2 = lds_ld(&sm.hv[16 + rs]);
      if (__all(h0 != SENT && h1 != SENT && h2 != SENT) || it >= SPIN_L) break;
      __builtin_amdgcn_s_sleep(1);
    }
#pragma unroll
    for (int e = 0; e < 4; ++e) {
      y[2 * e] = __uint_as_float(h0) * bflo(dw[0][e]) + __uint_as_float(h1) * bflo(dw[1][e]) + __uint_as_float(h2) * bflo(dw[2][e]);
      y[2 * e + 1] = __uint_as_float(h0) * bfhi(dw[0][e]) + __uint_as_float(h1) * bfhi(dw[1][e]) + __uint_as_float(h2) * bfhi(dw[2][e]);
    }
    if (w == 0) STAMP(13);
  }
  FENCE();
  if (!ow) {
    const int p0 = w - 9;
    u32 wb_;
    for (u32 it = 0;; ++it) {
      wb_ = lds_ld(&sm.wgt[(p0 + 7 * (l >> 4)) & 15]);
      if (__all(wb_ != SENT) || it >= SPIN_L) break;
      __builtin_amdgcn_s_sleep(1);
    }
    const unsigned char* op = ws + WS_OX + (u32)u * (16 * OXS) + (u32)p0 * OXS;
    u32 pend = p0 < 2 ? 7u : 3u;
    for (u32 it = 0; it < SPIN_G && pend; ++it) {
      u32x16 v[3];
      if (pend & 1u) asm volatile("s_load_dwordx16 %0, %1, 0x0 glc" : "=&s"(v[0]) : "s"(op) : "memory");
      if (pend & 2u) asm volatile("s_load_dwordx16 %0, %1, %2 glc" : "=&s"(v[1]) : "s"(op), "n"(7 * OXS) : "memory");
      if (pend & 4u) asm volatile("s_load_dwordx16 %0, %1, %2 glc" : "=&s"(v[2]) : "s"(op), "n"(14 * OXS) : "memory");
      asm volatile("s_waitcnt lgkmcnt(0)" ::: "memory");
#pragma unroll
      for (int k = 0; k < 3; ++k) {
        if (pend & (1u << k)) {
          asm volatile("" : "+s"(v[k]));
          u32 mx = v[k][0];
#pragma unroll
          for (int e = 1; e < 16; ++e) mx = mx > v[k][e] ? mx : v[k][e];
          if (mx != SENT) {
            pend &= ~(1u << k);
            u32 t = 0;
#pragma unroll
            for (int e = 0; e < 16; ++e) asm("v_writelane_b32 %0, %1, %2" : "+v"(t) : "s"(v[k][e]), "n"(16 * k + e));
            const u32 r = nosent(__uint_as_float(t) * __uint_as_float(wb_));
            const int p = p0 + 7 * k;
            if ((l >> 4) == k) lds_st(&sm.op[(((l >> 1) & 7) * 8 + (p >> 1)) * 4 + (l & 1) * 2 + (p & 1)], r);
          }
        }
      }
    }
    if (pend) { lds_st(&sm.op[4 * l], 0u); lds_st(&sm.op[4 * l + 1], 0u); lds_st(&sm.op[4 * l + 2], 0u); lds_st(&sm.op[4 * l + 3], 0u); }
    if (w == 9) STAMP(20);
    if (w == 13) STAMP(15);
  }
  if (ow) {
    float wf[16];
#pragma unroll
    for (int e = 0; e < 4; ++e) { wf[2 * e] = bflo(dw[3][e]); wf[2 * e + 1] = bfhi(dw[3][e]); wf[8 + 2 * e] = bflo(dw[4][e]); wf[9 + 2 * e] = bfhi(dw[4][e]); }
    if (w == 0) STAMP(21);
    u32x4 o4;
    for (u32 it = 0;; ++it) {
      o4 = lds_ld4(&sm.op[4 * l]);
      if (__all(o4[0] != SENT && o4[1] != SENT && o4[2] != SENT && o4[3] != SENT) || it >= SPIN_L) break;
    }
    float o0 = __uint_as_float(o4[0]) + __uint_as_float(o4[1]), o1 = __uint_as_float(o4[2]) + __uint_as_float(o4[3]);
    o0 += dpp<0xB1>(o0); o1 += dpp<0xB1>(o1);
    o0 += dpp<0x4E>(o0); o1 += dpp<0x4E>(o1);
    o0 += dpp<0x141>(o0); o1 += dpp<0x141>(o1);
#pragma unroll
    for (int e = 0; e < 8; ++e) y[e] += o0 * wf[e] + o1 * wf[8 + e];
    float a4[4], a2[2];
#pragma unroll
    for (int e = 0; e < 4; ++e) a4[e] = red32(y[e], y[e + 4]);
#pragma unroll
    for (int e = 0; e < 2; ++e) a2[e] = red16(a4[e], a4[e + 2]);
    const float t_ = (l & 8) ? a2[0] : a2[1], kp_ = (l & 8) ? a2[1] : a2[0];
    const float a1 = kp_ + dpp<0x128>(t_);
    __builtin_amdgcn_raw_buffer_store_b32(nosent(a1), rWS, WS_FX + (u32)(4 * w + ((l >> 1) & 3)) * 16384u + (u32)u * 64u +
                                          (8 * (l & 1) + 4 * (l >> 5) + 2 * ((l >> 4) & 1) + ((l >> 3) & 1)) * 4, 0, AUX_SC1);
    if (w == 0) STAMP(14);
  }
  if (fcons) {
    if (w >= 9) {
      for (u32 it = 0; it < SPIN_L; ++it) {
        const u32x4 o4 = lds_ld4(&sm.op[4 * l]);
        if (__all(o4[0] != SENT && o4[1] != SENT && o4[2] != SENT && o4[3] != SENT)) break;
        __builtin_amdgcn_s_sleep(2);
      }
    }
#define FDELAY 4
    __builtin_amdgcn_s_sleep(FDELAY);
    const int col = 4 * (l & 3) + 2 * (l >> 5) + ((l >> 4) & 1);
    const u32 xo = *((lds_u16*)sm.xs + cidx * 16 + col);
    u32 fa = WS_FX + cidx * 16384u + (u32)w * 1024u + l * 16;
    u32x4 t = {0u, 0u, 0u, 0u};
    for (u32 it = 0; it < SPIN_G; ++it) {
      asm volatile("" ::: "memory");
      const u32x4 r = __builtin_amdgcn_raw_buffer_load_b128(rWS, fa, 0, AUX_SC1);
      if (fa != OOR && r[0] != SENT && r[1] != SENT && r[2] != SENT && r[3] != SENT) { t = r; fa = OOR; }
      if (__all(fa == OOR)) break;
    }
#define FRED(t) ({ float c_ = red16(red32(__uint_as_float(t[0]), __uint_as_float(t[2])), red32(__uint_as_float(t[1]), __uint_as_float(t[3]))); \
      c_ += dpp<0x124>(c_); c_ + dpp<0x128>(c_); })
    {
      const float c = FRED(t);
      if ((l & 12) == 0) lds_st(&sm.fin[w][col], nosent(c));
    }
    if (w == 0) STAMP(22);
    u32x4 f0;
    for (u32 it = 0;; ++it) {
      f0 = lds_ld4(&sm.fin[0][4 * l]);
      if (__all(f0[0] != SENT && f0[1] != SENT && f0[2] != SENT && f0[3] != SENT) || it >= SPIN_L) break;
      __builtin_amdgcn_s_sleep(1);
    }
    const float c = FRED(f0);
    const __bf16 r = (__bf16)(bflo(xo) + c);
    if ((l & 12) == 0) out[cidx * 16 + col] = __builtin_bit_cast(unsigned short, r);
    if (cidx == 0 && l == 0) __builtin_amdgcn_raw_buffer_store_b32(sm.ev + 1u, rWS, WS_E, 0, AUX_SC1);
    if (w == 0) STAMP(23);
  }
}

extern "C" void kernel_launch(void* const* d_in, const int* in_sizes, int n_in, void* d_out, int out_size, void* d_ws, size_t ws_size,
                              hipStream_t stream) {
  fused_decode<<<256, 1024, 0, stream>>>((const unsigned short*)d_in[0], (const unsigned short*)d_in[1], (const unsigned short*)d_in[2],
                                         (const unsigned short*)d_in[3], (const unsigned short*)d_in[4], (const unsigned short*)d_in[6],
                                         (const unsigned short*)d_in[7], (const unsigned short*)d_in[8], (unsigned char*)d_ws,
                                         (unsigned short*)d_out);
}
```

```cpp
#include <hip/hip_runtime.h>
#include <hip/hip_bf16.h>
#include <cstdint>

typedef unsigned u32;
typedef u32 u32x4 __attribute__((ext_vector_type(4)));
typedef u32 u32x2 __attribute__((ext_vector_type(2)));
typedef u32 u32x16 __attribute__((ext_vector_type(16)));
typedef float f32x4 __attribute__((ext_vector_type(4)));
typedef float f32x2 __attribute__((ext_vector_type(2)));
typedef __bf16 bf16x2 __attribute__((ext_vector_type(2)));
typedef __bf16 bf16x8 __attribute__((ext_vector_type(8)));
typedef __amdgpu_buffer_rsrc_t rsrc_t;

#define WIN 3

constexpr u32 WS_E = 0;
constexpr u32 WS_QX = 4096;
constexpr u32 WS_HX = WS_QX + 256 * 512;
constexpr u32 WS_FL = WS_HX + 256 * 1024;
#define OXS 64
constexpr u32 WS_AG = WS_FL + 256 * 128;
constexpr u32 WS_ML = WS_AG + 16 * 512;
constexpr u32 WS_OX = WS_ML + 256 * 128;
constexpr u32 WS_FX = WS_OX + 256 * 16 * OXS;
#define XMARK 0
constexpr u32 WS_END = WS_FX + (36 + XMARK) * 16384;
constexpr u32 SPIN_G = 40000;
constexpr u32 SPIN_L = 4000000;
constexpr u32 SENT = 0xFFFFFFFFu;
constexpr u32 OOR = 0xFFF00000u;

#define STAMP(i) do { } while (0)
#define WST(id) do { } while (0)

#define AUX_NT 2
#define AUX_SC1 16
#define FENCE() __builtin_amdgcn_sched_barrier(0)
#define THR(n) do { asm volatile("s_waitcnt vmcnt(%0)" :: "n"(n) : "memory"); FENCE(); } while (0)
#define THROTTLE() THR(WIN - 1)
#define LDSWAIT() asm volatile("s_waitcnt lgkmcnt(0)" ::: "memory")

__device__ __forceinline__ float bflo(u32 u) { return __uint_as_float(u << 16); }
__device__ __forceinline__ float bfhi(u32 u) { return __uint_as_float(u & 0xffff0000u); }
__device__ __forceinline__ float rbf(float a) { return bflo(__builtin_bit_cast(unsigned short, (__bf16)a)); }
template <int CTRL> __device__ __forceinline__ float dpp(float x) {
  return __int_as_float(__builtin_amdgcn_update_dpp(0, __float_as_int(x), CTRL, 0xf, 0xf, true));
}
template <int N> __device__ __forceinline__ float rorf(float x) {
  if constexpr ((N & 15) == 0) return x; else return dpp<0x120 + (N & 15)>(x);
}
template <int N> __device__ __forceinline__ u32 roru(u32 x) {
  if constexpr ((N & 15) == 0) return x; else return (u32)__builtin_amdgcn_update_dpp(0, (int)x, 0x120 + (N & 15), 0xf, 0xf, true);
}
__device__ __forceinline__ float red32(float a, float b) {
  auto r = __builtin_amdgcn_permlane32_swap(__float_as_uint(a), __float_as_uint(b), false, false);
  return __uint_as_float(r[0]) + __uint_as_float(r[1]);
}
__device__ __forceinline__ float red16(float a, float b) {
  auto r = __builtin_amdgcn_permlane16_swap(__float_as_uint(a), __float_as_uint(b), false, false);
  return __uint_as_float(r[0]) + __uint_as_float(r[1]);
}
__device__ __forceinline__ float max32(float a) {
  auto r = __builtin_amdgcn_permlane32_swap(__float_as_uint(a), __float_as_uint(a), false, false);
  return fmaxf(__uint_as_float(r[0]), __uint_as_float(r[1]));
}
__device__ __forceinline__ float max16(float a) {
  auto r = __builtin_amdgcn_permlane16_swap(__float_as_uint(a), __float_as_uint(a), false, false);
  return fmaxf(__uint_as_float(r[0]), __uint_as_float(r[1]));
}
__device__ __forceinline__ float rowsum16(float s) {
  s += dpp<0xB1>(s); s += dpp<0x4E>(s); s += dpp<0x141>(s); s += dpp<0x140>(s);
  return s;
}
__device__ __forceinline__ float rowmax16(float s) {
  s = fmaxf(s, dpp<0xB1>(s)); s = fmaxf(s, dpp<0x4E>(s)); s = fmaxf(s, dpp<0x141>(s)); s = fmaxf(s, dpp<0x140>(s));
  return s;
}
__device__ __forceinline__ float wavesum(float s) { s = rowsum16(s); s = red16(s, s); s = red32(s, s); return s; }
__device__ __forceinline__ rsrc_t mkr(const void* p, u32 n) { return __builtin_amdgcn_make_buffer_rsrc((void*)p, 0, n, 0x00020000); }
__device__ __forceinline__ u32x4 gran(float a, float b, u32 tag) { u32x4 r = {__float_as_uint(a), tag, __float_as_uint(b), tag}; return r; }
__device__ __forceinline__ bool tagok(u32x4 v, u32 tag) { return v[1] == tag && v[3] == tag; }
typedef __attribute__((address_space(3))) volatile u32 lds_u32;
typedef __attribute__((address_space(3))) volatile u32x4 lds_u32x4;
typedef __attribute__((address_space(3))) volatile unsigned short lds_u16;
__device__ __forceinline__ u32 lds_ld(u32* p) { return *(lds_u32*)p; }
__device__ __forceinline__ u32x4 lds_ld4(u32* p) { return *(lds_u32x4*)p; }
__device__ __forceinline__ void lds_st(u32* p, u32 v) { *(lds_u32*)p = v; }
#define PIN8(a) asm volatile("" : "+v"(a[0]), "+v"(a[1]), "+v"(a[2]), "+v"(a[3]), "+v"(a[4]), "+v"(a[5]), "+v"(a[6]), "+v"(a[7]))
__device__ __forceinline__ u32 nosent(float v) { const u32 t = __float_as_uint(v); return t == SENT ? 0x7FC00000u : t; }
__device__ __forceinline__ u32 arrive(u32* c, int l) {
  u32 old = 0;
  LDSWAIT();
  if (l == 0) old = __hip_atomic_fetch_add(c, 1u, __ATOMIC_RELAXED, __HIP_MEMORY_SCOPE_WORKGROUP);
  old = __builtin_amdgcn_readfirstlane(old);
  asm volatile("" ::: "memory");
  return old;
}
__device__ __forceinline__ f32x4 mfma(u32x4 a, u32x4 b, f32x4 c) {
  return __builtin_amdgcn_mfma_f32_16x16x32_bf16(__builtin_bit_cast(bf16x8, a), __builtin_bit_cast(bf16x8, b), c, 0, 0, 0);
}

struct __align__(16) Smem {
  float qp[8][64];
  float hs[12][64];
  float acc[8][256];
  float wmax[2][8];
  float wl[2][8];
  u32 qv[2][2][64];
  u32 xs[320];
  float xn[640];
  u32 op[256];
  u32 fin[16][16];
  u32 hv[24];
  u32 wgt[16];
  u32 cq, ch, cm, ca, fq, fx, cv, ff;
  u32 an, pad3, pad4, ev;
};
#define WA 3
#define NA 3
#define WB 6
#define NB 32
#define WC 6
#define NC 32
#define WD 6
#define BARI 2
#define TRIGN 8u
#define QSLP 4
#define HSLP 8
#define HSLP0 32
#define AUX_1ST 0
#define MSLP 4
#define MSLP0 16
#define AUX_E 0
#define WS_ 3
#define QPOS 7
#define TRIG 15
#define PMODE 0
#define PIT 4
#define PTH 1
#define PACE_AT(base, T) do {                                                                                               \
      asm volatile("1: s_memrealtime s[94:95]\n s_waitcnt lgkmcnt(0)\n s_sub_u32 s94, s94, %0\n s_cmpk_lt_i32 s94, %1\n"    \
                   " s_cbranch_scc0 2f\n s_sleep 1\n s_branch 1b\n2:"                                                      \
                   :: "s"(base), "n"(T) : "s94", "s95", "scc", "memory");                                                   \
      FENCE(); } while (0)
#define PN0 3
#define PT0 2150
#define N1 4
#define N2 17
#define TAU1 285
#define TAU2 400
#define TAU3 285
constexpr int TK(int n) { return n <= N1 ? PT0 + (n - PN0) * TAU1 : n <= N2 ? TK(N1) + (n - N1) * TAU2 : TK(N2) + (n - N2) * TAU3; }
#define GT0 2600
#define GTAU 800
#define DOFF 700
#define DTAU 400
#define DSTAG 0
#define AOFF 0
#define ACL 100
#define ANCHOR(dst) do { const u32 a_ = lds_ld(&sm.an);                                                                     \
      dst = __builtin_amdgcn_readfirstlane(a_ ? tb + (u32)min(max((int)(a_ - tb), -ACL), ACL) : tb); FENCE(); } while (0)
#define BUMP(c) do { if (l == 0) __hip_atomic_fetch_add(&sm.c, 1u, __ATOMIC_RELAXED, __HIP_MEMORY_SCOPE_WORKGROUP); } while (0)
#define LWAIT(c, n) do { for (u32 it = 0; lds_ld(&sm.c) != (n) && it < SPIN_L; ++it) __builtin_amdgcn_s_sleep(1); asm volatile("" ::: "memory"); } while (0)

__global__ void __launch_bounds__(1024, 4)
fused_decode(const unsigned short* __restrict__ x, const unsigned short* __restrict__ Kc, const unsigned short* __restrict__ Vc,
             const unsigned short* __restrict__ wq, const unsigned short* __restrict__ wo, const unsigned short* __restrict__ wg,
             const unsigned short* __restrict__ wu, const unsigned short* __restrict__ wd, unsigned char* __restrict__ ws,
             unsigned short* __restrict__ out) {
  __shared__ Smem sm;
  const u32 tb = (u32)__builtin_amdgcn_s_memrealtime();
#define CPF 1
#define CODEB 17408u
#define CPS 8u
  u32 cpf = 0u;
  {
    typedef const __attribute__((address_space(1))) unsigned char* gptr;
    gptr pc;
    asm volatile("s_getpc_b64 %0" : "=s"(pc));
    const u32 po = ((1023u - threadIdx.x) * CPS + ((blockIdx.x >> 3) & (CPS - 1u))) * 128u;
    if (po <= CODEB) cpf = *(const __attribute__((address_space(1))) u32*)(pc + po);
  }
  const int b = blockIdx.x;
  const int w = __builtin_amdgcn_readfirstlane(threadIdx.x >> 6);
  const int l = threadIdx.x & 63;
#define PXA 0
#define PXB 0
  const int u = ((b + PXA) & 7) * 32 + ((b >> 3) ^ PXB);
  const int g = u >> 4, j = u & 15, k4 = u & 3;
  const bool fcons = b < 36;
  const u32 cidx = (u32)b;
  FENCE();
  asm volatile("" :: "s"(x), "s"(Kc), "s"(Vc), "s"(wq), "s"(wo), "s"(wg), "s"(wu), "s"(wd), "s"(ws));
  const rsrc_t rWS = mkr(ws, WS_END);
  const bool ow = w < 9;
  const rsrc_t rD = mkr(wd, 4608u * 1152u), rO = mkr(wo, 4096u * 1152u);
  const u32 co = (u32)w * 128u + (l & 7) * 16;
  const u32 od = ow ? (u32)(18 * u + (l >> 3)) * 1152u + co : OOR;
  const u32 oo = ow ? (u32)(16 * u + (l >> 3)) * 1152u + co : OOR;
  u32x4 dw[5];
#define DWLOADS() do {                                                                                     \
    dw[0] = __builtin_amdgcn_raw_buffer_load_b128(rD, od, 0, AUX_NT);                                      \
    dw[1] = __builtin_amdgcn_raw_buffer_load_b128(rD, od, 8 * 1152, AUX_NT);                               \
    dw[2] = __builtin_amdgcn_raw_buffer_load_b128(rD, l < 16 ? od : OOR, 16 * 1152, AUX_NT);               \
    dw[3] = __builtin_amdgcn_raw_buffer_load_b128(rO, oo, 0, AUX_NT);                                      \
    dw[4] = __builtin_amdgcn_raw_buffer_load_b128(rO, oo, 8 * 1152, AUX_NT); FENCE(); } while (0)

  if (w < 8) {
    const u32 ts0 = tb + (u32)(w * (GTAU / 60));
    const rsrc_t rX = mkr(x, 1152), rQ = mkr(wq, 576u * 8192u), rG = mkr((g & 1) ? wu : wg, 576u * 9216u);
    const int rb = 144 * k4 + 18 * w;
    const u32 qo = (u32)(rb + (l >> 3)) * 8192u + (u32)(256 * g + 64 * (j >> 2)) * 2 + (l & 7) * 16;
    const int uB = 9 * j + 3 * (w >> 1) + 1, uA = uB - 1 + 2 * (w & 1);
    const int rA = 36 * (uA & 15), rB = 36 * (uB & 15) + 18 * (w & 1);
    const u32 gv = w < 6 ? (u32)(l >> 3) * 9216u + (u32)(u >> 5) * 1152u + (l & 7) * 16 : OOR;
    const u32 gB = w < 6 ? (u32)(rB * 9216 + (uB >> 4) * 128) : 0u, gA = (u32)(rA * 9216 + (uA >> 4) * 128);
#define AUX_G AUX_NT
    u32x4 xa = {0u, 0u, 0u, 0u};
    u32 xb = 0u, Ev = 0u;
    if (w == 0) {
      xa = __builtin_amdgcn_raw_buffer_load_b128(rX, l * 16, 0, 0);
      xb = __builtin_amdgcn_raw_buffer_load_b32(rX, 1024 + l * 4, 0, 0);
      Ev = __builtin_amdgcn_raw_buffer_load_b32(rWS, WS_E, 0, AUX_E);
    }
    {
      const u32x4 sv = {SENT, SENT, SENT, SENT};
      if (w == 7) __builtin_amdgcn_raw_buffer_store_b128(sv, rWS, WS_OX + (u32)u * (16 * OXS) + (l >> 2) * OXS + (l & 3) * 16, 0, AUX_SC1);
      if (w == 6) __builtin_amdgcn_raw_buffer_store_b128(sv, rWS, l < 8 ? WS_ML + (u32)u * 128u + l * 16 : OOR, 0, AUX_SC1);
    }
    FENCE();
    u32x4 fq_[3], fg[7];
    fq_[0] = __builtin_amdgcn_raw_buffer_load_b128(rQ, qo, 0, AUX_NT); FENCE();
    fq_[1] = __builtin_amdgcn_raw_buffer_load_b128(rQ, qo, 8 * 8192, AUX_NT); FENCE();
    fq_[2] = __builtin_amdgcn_raw_buffer_load_b128(rQ, l < 16 ? qo : OOR, 16 * 8192, AUX_NT); FENCE();
    if (b < 36 + XMARK) {
      const u32x4 sv = {SENT, SENT, SENT, SENT};
#pragma unroll
      for (int i = 0; i < 2; ++i) __builtin_amdgcn_raw_buffer_store_b128(sv, rWS, WS_FX + cidx * 16384u + (u32)w * 2048u + l * 16, i * 1024, AUX_SC1);
    }
    FENCE();
    if (threadIdx.x < 281) {
      const u32 iv = threadIdx.x < 276 ? SENT : 0u;
      *(u32x2*)&sm.op[2 * threadIdx.x] = u32x2{iv, iv};
    }
    LDSWAIT(); __builtin_amdgcn_s_barrier(); FENCE(); WST(0);
    fg[0] = __builtin_amdgcn_raw_buffer_load_b128(rG, gv, gB, AUX_G); FENCE();
    if (w == 0) {
      float xf[10];
#pragma unroll
      for (int e = 0; e < 4; ++e) { xf[2 * e] = bflo(xa[e]); xf[2 * e + 1] = bfhi(xa[e]); }
      xf[8] = bflo(xb); xf[9] = bfhi(xb);
      const float mean = wavesum((((xf[0] + xf[1]) + (xf[2] + xf[3])) + ((xf[4] + xf[5]) + (xf[6] + xf[7]))) + (xf[8] + xf[9])) * (1.0f / 576.0f);
      const float mb = rbf(mean);
      float xd[10];
#pragma unroll
      for (int e = 0; e < 10; ++e) { xd[e] = rbf(xf[e] - mb); xf[e] -= mean; }
      float v0 = xf[0] * xf[0], v1 = xf[1] * xf[1];
#pragma unroll
      for (int e = 2; e < 8; e += 2) { v0 += xf[e] * xf[e]; v1 += xf[e + 1] * xf[e + 1]; }
      v0 += l < 32 ? xf[8] * xf[8] + xf[9] * xf[9] : 0.f;
      const float rs = __builtin_amdgcn_rcpf(rbf(__builtin_amdgcn_sqrtf(rbf(rbf(wavesum(v0 + v1) * (1.0f / 576.0f)) + 1e-5f))));
#pragma unroll
      for (int e = 0; e < 10; ++e) xf[e] = rbf(xd[e] * rs);
      *(f32x4*)&sm.xn[8 * l] = f32x4{xf[0], xf[1], xf[2], xf[3]};
      *(f32x4*)&sm.xn[8 * l + 4] = f32x4{xf[4], xf[5], xf[6], xf[7]};
      *(f32x2*)&sm.xn[512 + 2 * l] = f32x2{xf[8], xf[9]};
      *(u32x4*)&sm.xs[4 * l] = xa;
      sm.xs[256 + l] = xb;
      if (l == 0) sm.ev = Ev;
      LDSWAIT();
      if (l == 0) lds_st(&sm.fx, 1u);
      STAMP(2);
    }
    FENCE();
    LWAIT(fx, 1u);
    const u32 tag = __builtin_amdgcn_readfirstlane(sm.ev) * 0x9E3779B1u + 0x7F4A7C15u;
    float xq[3], xg[8];
#pragma unroll
    for (int i = 0; i < 3; ++i) xq[i] = sm.xn[rb + (l >> 3) + 8 * i];
    asm volatile("" : "+v"(xq[0]), "+v"(xq[1]), "+v"(xq[2]));
    FENCE();
    THR(1);
    WST(1);
#define FACC(N, G, F, X) do {                                                                                          \
      _Pragma("unroll") for (int e = 0; e < 4; ++e) G[e] = f32x2{X[0], X[0]} * f32x2{bflo(F[0][e]), bfhi(F[0][e])};    \
      _Pragma("unroll") for (int i = 1; i < N; ++i)                                                                    \
        _Pragma("unroll") for (int e = 0; e < 4; ++e) G[e] += f32x2{X[i], X[i]} * f32x2{bflo(F[i][e]), bfhi(F[i][e])}; \
    } while (0)
    {
      f32x2 g2[4];
      FACC(3, g2, fq_, xq);
      float a4[4], a2[2];
#pragma unroll
      for (int e = 0; e < 4; ++e) a4[e] = red32(g2[e >> 1][e & 1], g2[2 + (e >> 1)][e & 1]);
#pragma unroll
      for (int e = 0; e < 2; ++e) a2[e] = red16(a4[e], a4[e + 2]);
      const float t_ = (l & 8) ? a2[0] : a2[1], kp_ = (l & 8) ? a2[1] : a2[0];
      sm.qp[w][8 * (l & 7) + 4 * (l >> 5) + 2 * ((l >> 4) & 1) + ((l >> 3) & 1)] = kp_ + dpp<0x128>(t_);
      if (w == 0) STAMP(1);
      if (arrive(&sm.cq, l) == 7u) {
        const int P_ = l & 31, hf = l >> 5;
        const f32x2 s_ = (*(const f32x2*)&sm.qp[hf * 4][2 * P_] + *(const f32x2*)&sm.qp[hf * 4 + 1][2 * P_]) +
                         (*(const f32x2*)&sm.qp[hf * 4 + 2][2 * P_] + *(const f32x2*)&sm.qp[hf * 4 + 3][2 * P_]);
        const float s0 = red32(s_[0], s_[0]), s1 = red32(s_[1], s_[1]);
        if (l < 32) __builtin_amdgcn_raw_buffer_store_b128(gran(s0, s1, tag), rWS, WS_QX + (u32)u * 512u + P_ * 16, 0, AUX_SC1);
        __builtin_amdgcn_raw_buffer_store_b32(tag, rWS, l < 16 ? WS_FL + (u32)(g * 16 + l) * 128u + j * 4 : OOR, 0, AUX_SC1);
        if (fcons) __builtin_amdgcn_raw_buffer_store_b64(u32x2{tb, tag}, rWS, l < 16 ? WS_AG + (u32)l * 512u + cidx * 8u : OOR, 0, AUX_SC1);
        STAMP(3);
      }
      FENCE();
    }
    if (w < 6) {
      const float* const xr = &sm.xn[l >> 3];
      xg[0] = xr[rB]; xg[1] = xr[rB + 8];
      xg[2] = xr[l < 16 ? rB + 16 : rA - 2];
      xg[4] = xr[rA + 6]; xg[5] = xr[rA + 14]; xg[6] = xr[rA + 22]; xg[7] = xr[rA + 30];
      xg[7] = l < 48 ? xg[7] : 0.f;
      xg[3] = l < 16 ? 0.f : xg[2];
      xg[2] = l < 16 ? xg[2] : 0.f;
      const int c0 = (9 * j) >> 4, n0 = 16 - ((9 * j) & 15);
      const int s0 = j - ((c0 * 455) >> 8);
      const int c1 = n0 < 9 ? c0 + 1 : j == 0 ? 0 : j == 6 ? 4 : 8, s1 = n0 < 9 ? 0 : 2;
      const u32 cA = (u32)(64 * c0 + l), cB = (u32)(64 * c1 + l), qA = (cA * 3641u) >> 16, qB = (cB * 3641u) >> 16;
      const u32 hbase = WS_HX + (u32)(u & ~31) * 1024u + (u32)(g & 1) * 432u;
      u32 hA = hbase + (u32)s0 * 144u + qA * 1024u + (cA - 18u * qA) * 8u;
      u32 hB = (n0 < 9 || j == 0 || j == 6 || j == 15) ? hbase + (u32)s1 * 144u + qB * 1024u + (cB - 18u * qB) * 8u : OOR;
      u32 gv2 = gv + (l < 16 ? gB + 16u * 9216u : gA - 2u * 9216u), gv6 = l < 48 ? gv : OOR;
      PIN8(xg);
      asm volatile("" : "+v"(hA), "+v"(hB), "+v"(gv2), "+v"(gv6));
      const int cl = 8 * (l & 7) + 4 * (l >> 5) + 2 * ((l >> 4) & 1) + ((l >> 3) & 1);
#define GLD(k, vo, so) do { PACE_AT(ts0, (GT0 + ((k) - 1) * GTAU) / 10); fg[k] = __builtin_amdgcn_raw_buffer_load_b128(rG, vo, so, AUX_G); FENCE(); } while (0)
#define COLSUM(G) ({ float a4_[4], a2_[2];                                                                              \
      _Pragma("unroll") for (int e = 0; e < 4; ++e) a4_[e] = red32(G[e >> 1][e & 1], G[2 + (e >> 1)][e & 1]);           \
      _Pragma("unroll") for (int e = 0; e < 2; ++e) a2_[e] = red16(a4_[e], a4_[e + 2]);                                 \
      const float t_ = (l & 8) ? a2_[0] : a2_[1], kp_ = (l & 8) ? a2_[1] : a2_[0];                                      \
      kp_ + dpp<0x128>(t_); })
      GLD(1, gv, gB + 8u * 9216u); GLD(2, gv2, 0); GLD(3, gv, gA + 6u * 9216u); GLD(4, gv, gA + 14u * 9216u);
      {
        f32x2 g2[4];
        FACC(3, g2, fg, xg);
        sm.hs[2 * (w & 6) + 1 + (w & 1)][cl] = COLSUM(g2);
        FENCE();
      }
      GLD(5, gv, gA + 22u * 9216u); GLD(6, gv6, gA + 30u * 9216u);
      {
        f32x2 g2[4];
        FACC(5, g2, (fg + 2), (xg + 3));
        sm.hs[2 * (w & 6) + 3 * (w & 1)][cl] = COLSUM(g2);
        WST(2);
        if (arrive(&sm.ch, l) == 5u) {
          float t[12], a0 = 0.f, a1 = 0.f;
#pragma unroll
          for (int i = 0; i < 12; ++i) t[i] = sm.hs[i][l];
#pragma unroll
          for (int i = 0; i < 12; ++i) {
            const bool first = 3 * (i >> 2) + ((i & 3) + 1) / 2 < n0;
            a0 += first ? t[i] : 0.f; a1 += first ? 0.f : t[i];
          }
          const u32x2 r0 = {__float_as_uint(a0), tag}, r1 = {__float_as_uint(a1), tag};
          __builtin_amdgcn_raw_buffer_store_b64(r0, rWS, hA, 0, AUX_SC1);
          __builtin_amdgcn_raw_buffer_store_b64(r1, rWS, hB, 0, AUX_SC1);
          STAMP(6);
        }
        FENCE();
      }
    } else {
#define TOUCH 1
#define AUX_T 0
      u32 tc0, tc1 = 0u;
      if (w == 6) {
        const rsrc_t rV = mkr(Vc, 33554432u);
        constexpr u32 TG = TOUCH == 1 ? 65536u : TOUCH == 2 ? 16384u : 4096u;
        tc0 = __builtin_amdgcn_raw_buffer_load_b32(rV, l < (int)(131072u / TG) ? (u32)u * 131072u + (u32)l * TG : OOR, 0, AUX_T);
      } else {
        constexpr int TN = TOUCH == 3 ? 6 : 2;
        tc0 = __builtin_amdgcn_raw_buffer_load_b32(rD, l < TN ? (u32)(18 * u) * 1152u + (u32)l * (20608u / (TN - 1)) / 128u * 128u : OOR, 0, AUX_T);
        tc1 = __builtin_amdgcn_raw_buffer_load_b32(rO, l < TN ? (u32)(16 * u) * 1152u + (u32)l * (18304u / (TN - 1)) / 128u * 128u : OOR, 0, AUX_T);
      }
      FENCE();
      if (w == 6) {
        const unsigned char* fp = ws + WS_FL + (u32)u * 128u;
        LWAIT(cq, 8u);
        for (u32 it = 0; it < SPIN_G; ++it) {
          u32x16 v;
          __builtin_amdgcn_s_sleep(QSLP);
          asm volatile("s_load_dwordx16 %0, %1, 0x0 glc\n s_waitcnt lgkmcnt(0)" : "=&s"(v) : "s"(fp) : "memory");
          u32 bad = 0;
#pragma unroll
          for (int e = 0; e < 16; ++e) bad |= v[e] ^ tag;
          if (bad == 0) break;
        }
        if (l == 0) lds_st(&sm.ff, 1u);
      } else {
        LWAIT(ff, 1u);
      }
      if (w == 6) STAMP(4);
      const u32 g1a = WS_QX + (u32)(g * 16 + (w - 6) * 8 + (l >> 4)) * 512u + (l & 15) * 16;
      u32x4 g1[4];
#pragma unroll
      for (int i = 0; i < 4; ++i) g1[i] = __builtin_amdgcn_raw_buffer_load_b128(rWS, g1a + (i & 1) * 256 + (i >> 1) * 2048, 0, AUX_1ST);
      asm volatile("" : "+v"(g1[0]), "+v"(g1[1]), "+v"(g1[2]), "+v"(g1[3]));
      asm volatile("" :: "v"(tc0), "v"(tc1));
      if (w == 6) STAMP(16);
      if (!__all(tagok(g1[0], tag) && tagok(g1[1], tag) && tagok(g1[2], tag) && tagok(g1[3], tag))) {
        for (u32 it = 0;; ++it) {
          asm volatile("" ::: "memory");
#pragma unroll
          for (int i = 0; i < 4; ++i) g1[i] = __builtin_amdgcn_raw_buffer_load_b128(rWS, g1a + (i & 1) * 256 + (i >> 1) * 2048, 0, AUX_SC1);
          asm volatile("" : "+v"(g1[0]), "+v"(g1[1]), "+v"(g1[2]), "+v"(g1[3]));
          if (__all(tagok(g1[0], tag) && tagok(g1[1], tag) && tagok(g1[2], tag) && tagok(g1[3], tag)) || it >= SPIN_G) break;
        }
      }
#pragma unroll
      for (int i = 0; i < 4; ++i) {
        float a = red16(__uint_as_float(g1[i][0]), __uint_as_float(g1[i][2]));
        a = red32(a, a);
        const u32 hib = __builtin_bit_cast(unsigned short, (__bf16)a);
        *((lds_u16*)&sm.qv[w - 6][l >> 5][16 * i + (l & 15)] + ((l >> 4) & 1)) = (unsigned short)(l < 32 ? hib : 0u);
      }
      LDSWAIT();
      BUMP(fq);
      if (w == 6) STAMP(5);
      WST(3);
    }
    FENCE();
    if (w == 7) {
      {
        const u32 ab = l < 18 ? WS_AG + (u32)g * 512u + l * 16 : OOR;
        u32x4 e0;
        for (u32 it = 0;; ++it) {
          asm volatile("" ::: "memory");
          e0 = __builtin_amdgcn_raw_buffer_load_b128(rWS, ab, 0, AUX_SC1);
          if (__all(l >= 18 || tagok(e0, tag)) || it >= SPIN_G) break;
          __builtin_amdgcn_s_sleep(8);
        }
        const int d0 = min(max((int)(e0[0] - tb), -200), 200), d1 = min(max((int)(e0[2] - tb), -200), 200);
        const float dm = rowsum16((float)(d0 + d1)) * (1.0f / 32.0f);
        if (l == 0) lds_st(&sm.an, (tb + (u32)((int)(dm + 256.5f) - 256 + AOFF)) | 1u);
        STAMP(19);
      }
      const bool on = (l & 15) < 9 && l < 48;
      const u32 hb = on ? WS_HX + (u32)u * 1024u + (u32)(l >> 4) * 144u + (l & 15) * 16 : OOR;
      const unsigned char* hs = ws + WS_HX + (u32)u * 1024u;
      LWAIT(ch, 6u);
      for (u32 it = 0; it < SPIN_G; ++it) {
        u32 a0, a6; u32x4 a1, a2, a3, a4, a5;
        __builtin_amdgcn_s_sleep(HSLP);
        asm volatile("s_load_dword %0, %7, 0x4 glc\n s_load_dwordx4 %1, %7, 0x8c glc\n s_load_dwordx4 %2, %7, 0x11c glc\n s_load_dwordx4 %3, %7, 0x1ac glc\n"
                     " s_load_dwordx4 %4, %7, 0x23c glc\n s_load_dwordx4 %5, %7, 0x2cc glc\n s_load_dword %6, %7, 0x35c glc\n s_waitcnt lgkmcnt(0)"
                     : "=&s"(a0), "=&s"(a1), "=&s"(a2), "=&s"(a3), "=&s"(a4), "=&s"(a5), "=&s"(a6) : "s"(hs) : "memory");
        if (((a0 ^ tag) | (a6 ^ tag) | (a1[0] ^ tag) | (a1[2] ^ tag) | (a2[0] ^ tag) | (a2[2] ^ tag) | (a3[0] ^ tag) | (a3[2] ^ tag) |
             (a4[0] ^ tag) | (a4[2] ^ tag) | (a5[0] ^ tag) | (a5[2] ^ tag)) == 0) break;
      }
      u32x4 t0, t1;
      for (u32 it = 0;; ++it) {
        asm volatile("" ::: "memory");
        t0 = __builtin_amdgcn_raw_buffer_load_b128(rWS, hb, 0, AUX_SC1);
        t1 = __builtin_amdgcn_raw_buffer_load_b128(rWS, hb + 432, 0, AUX_SC1);
        if (__all(!on || (tagok(t0, tag) && tagok(t1, tag))) || it >= SPIN_G) break;
        __builtin_amdgcn_s_sleep(8);
      }
      float gs = red16(__uint_as_float(t0[0]), __uint_as_float(t0[2])), us = red16(__uint_as_float(t1[0]), __uint_as_float(t1[2]));
      gs = red32(gs, gs); us = red32(us, us);
      const float hval = on ? gs / (1.0f + __expf(-gs)) * us : 0.f;
      if (l < 32 && (l & 15) < 12) lds_st(&sm.hv[2 * (l & 15) + (l >> 4)], nosent(hval));
      STAMP(10);
    } else if (w == 6) {
      LWAIT(cm, 8u);
      const int h = (l >> 3) & 1;
      const float mi = sm.wmax[h][l & 7], li = sm.wl[h][l & 7];
      float M = fmaxf(mi, dpp<0xB1>(mi)); M = fmaxf(M, dpp<0x4E>(M)); M = fmaxf(M, dpp<0x141>(M));
      float L = li * __builtin_amdgcn_exp2f(mi - M);
      L += dpp<0xB1>(L); L += dpp<0x4E>(L); L += dpp<0x141>(L);
      const u32x2 ml = {nosent(M), nosent(L)};
      __builtin_amdgcn_raw_buffer_store_b64(ml, rWS, l < 16 ? WS_ML + (u32)(g * 16 + l) * 128u + j * 8 : OOR, 0, AUX_SC1);
      STAMP(17);
      __builtin_amdgcn_s_sleep(MSLP0);
      const unsigned char* mp = ws + WS_ML + (u32)u * 128u;
      u32x16 v[2];
      for (u32 it = 0; it < SPIN_G; ++it) {
        asm volatile("s_load_dwordx16 %0, %1, 0x0 glc" : "=&s"(v[0]) : "s"(mp) : "memory");
        asm volatile("s_load_dwordx16 %0, %1, 0x40 glc" : "=&s"(v[1]) : "s"(mp) : "memory");
        asm volatile("s_waitcnt lgkmcnt(0)" ::: "memory");
        u32 mx = 0;
#pragma unroll
        for (int k = 0; k < 2; ++k) {
          asm volatile("" : "+s"(v[k]));
#pragma unroll
          for (int e = 0; e < 16; ++e) mx = mx > v[k][e] ? mx : v[k][e];
        }
        if (mx != SENT) break;
        __builtin_amdgcn_s_sleep(MSLP);
      }
      u32 tm = 0, tl = 0;
#pragma unroll
      for (int k = 0; k < 2; ++k)
#pragma unroll
        for (int e = 0; e < 8; ++e) {
          asm("v_writelane_b32 %0, %1, %2" : "+v"(tm) : "s"(v[k][2 * e]), "n"(8 * k + e));
          asm("v_writelane_b32 %0, %1, %2" : "+v"(tl) : "s"(v[k][2 * e + 1]), "n"(8 * k + e));
        }
      asm volatile("s_nop 1" : "+v"(tm), "+v"(tl));
      const float mi2 = __uint_as_float(tm);
      const float e = __builtin_amdgcn_exp2f(mi2 - rowmax16(mi2));
      const float L2 = rowsum16(e * __uint_as_float(tl));
      if (l < 16) lds_st(&sm.wgt[l], nosent(e / L2));
      STAMP(18);
    }
    FENCE();
    for (u32 it = 0; lds_ld(&sm.cv) < TRIGN && it < SPIN_L; ++it) __builtin_amdgcn_s_sleep(1);
    asm volatile("" ::: "memory");
    if (w == 0) STAMP(24);
    u32 td0;
    ANCHOR(td0);
    td0 += (u32)(w * (DSTAG / 10));
#define DPACE(k) PACE_AT(td0, (TK(31) + DOFF + (k) * DTAU) / 10)
    DPACE(0); dw[0] = __builtin_amdgcn_raw_buffer_load_b128(rD, od, 0, AUX_NT); FENCE();
    DPACE(1); dw[1] = __builtin_amdgcn_raw_buffer_load_b128(rD, od, 8 * 1152, AUX_NT); FENCE();
    DPACE(2); dw[3] = __builtin_amdgcn_raw_buffer_load_b128(rO, oo, 0, AUX_NT); FENCE();
    DPACE(3); dw[4] = __builtin_amdgcn_raw_buffer_load_b128(rO, oo, 8 * 1152, AUX_NT); FENCE();
    dw[2] = __builtin_amdgcn_raw_buffer_load_b128(rD, l < 16 ? od : OOR, 16 * 1152, AUX_NT); FENCE();
    if (w == 0) STAMP(7);
    FENCE();
  } else {
    const rsrc_t rK = mkr(Kc, 33554432u), rV = mkr(Vc, 33554432u);
#define LB0 8
#define LB1 9
#define WB0 14
#define WB1 15
#define WB2 16
#define TB0 10
#define TB1 11
#define TB2 12
#define TB3 13
    const u32 kvb = (u32)u * 131072u + (u32)(((w & 1) << WB0) | (((w >> 1) & 1) << WB1) | (((w >> 2) & 1) << WB2));
#define TOF(i) ((((i) & 1) << TB0) | ((((i) >> 1) & 1) << TB1) | ((((i) >> 2) & 1) << TB2) | ((((i) >> 3) & 1) << TB3))
#define TVO(i) (TOF(i) & 4095)
#define TSO(i) (TOF(i) & ~4095)
    const u32 voff = kvb + (u32)((((l >> 4) & 1) << LB0) | ((l >> 5) << LB1) | ((l & 15) * 16));
    const u32 koff = kvb + (u32)((((l >> 2) & 1) << LB0) | (((l >> 3) & 1) << LB1) | ((l >> 4) * 64 + (l & 3) * 16));
    u32x4 kk[16], vv[16];
#define WINOF(n) ((n) < NA ? WA : (n) < NB ? WB : (n) < NC ? WC : WD)
    u32 tk = tb;
#define PACE(n) do { if ((n) >= PN0) PACE_AT(tk, TK(n) / 10); } while (0)
#define KLD(i) do { if ((i) >= WA) THR(WINOF(i) - 1);                                                                      \
      PACE(i);                                                                                                              \
      kk[i] = __builtin_amdgcn_raw_buffer_load_b128(rK, koff + TVO(i), TSO(i), AUX_NT); FENCE();                            \
      if ((i) == BARI) { __builtin_amdgcn_s_barrier(); FENCE(); WST(0); } } while (0)
#define VLD(i) do { if ((i) % 3 == 1 && (i) < 12) ANCHOR(tk); THR(WINOF(16 + (i)) - 1);                                                                               \
      PACE(16 + (i));                                                                                                       \
      vv[i] = __builtin_amdgcn_raw_buffer_load_b128(rV, voff + TVO(i), TSO(i), AUX_NT); FENCE();                            \
      if ((i) == TRIG) { BUMP(cv); FENCE(); } } while (0)
    KLD(0); KLD(1); KLD(2); KLD(3); KLD(4); KLD(5); KLD(6); KLD(7);
    asm volatile("" :: "v"(cpf));
    WST(1);
    KLD(8); KLD(9); KLD(10); KLD(11); KLD(12); KLD(13); KLD(14); KLD(15);
    WST(2);
    VLD(0); VLD(1); VLD(2); VLD(3); VLD(4); VLD(5);
    if (QPOS >= 6) VLD(6);
    if (QPOS >= 7) VLD(7);
    WST(3);
    LWAIT(fq, 2u);
    if (w == 8) STAMP(8);
    WST(4);
    const int bank = (l >> 2) & 3;
    u32 pk[4];
    float mw;
    {
      const u32x4 qb = *(const u32x4*)&sm.qv[(l >> 1) & 1][l & 1][4 * ((l >> 4) * 4 + bank)];
      float sc[4];
#pragma unroll
      for (int ab = 0; ab < 4; ++ab) {
        const f32x4 z = {0.f, 0.f, 0.f, 0.f};
        const f32x4 d0 = mfma(kk[4 * ab], qb, z), d1 = mfma(kk[4 * ab + 1], qb, z), d2 = mfma(kk[4 * ab + 2], qb, z), d3 = mfma(kk[4 * ab + 3], qb, z);
        float a0 = ((d0[0] + rorf<-4>(d0[1])) + rorf<-8>(d0[2])) + rorf<-12>(d0[3]);
        float a1 = ((d1[1] + rorf<4>(d1[0])) + rorf<-4>(d1[2])) + rorf<-8>(d1[3]);
        float a2 = ((d2[2] + rorf<8>(d2[0])) + rorf<4>(d2[1])) + rorf<-4>(d2[3]);
        float a3 = ((d3[3] + rorf<12>(d3[0])) + rorf<8>(d3[1])) + rorf<4>(d3[2]);
        asm volatile("" : "+v"(a0), "+v"(a1), "+v"(a2), "+v"(a3));
        const float t01 = (l & 4) ? a1 : a0, t23 = (l & 4) ? a3 : a2;
        const float t = (l & 8) ? t23 : t01;
        sc[ab] = rbf(rbf(t + dpp<0xB1>(t)) * 0.08838834764831845f) * 1.4426950408889634f;
        asm volatile("" : "+v"(sc[ab]));
        if (ab == 1) { FENCE(); VLD(QPOS + 1); }
      }
      FENCE(); VLD(QPOS + 2);
      float m = fmaxf(fmaxf(sc[0], sc[1]), fmaxf(sc[2], sc[3]));
      m = fmaxf(m, rorf<4>(m)); m = fmaxf(m, rorf<8>(m));
      m = max32(max16(m));
      mw = m;
#pragma unroll
      for (int ab = 0; ab < 4; ++ab) {
        const float p = __builtin_amdgcn_exp2f(sc[ab] - m);
        const bf16x2 t = {(__bf16)p, (__bf16)p};
        pk[ab] = __builtin_bit_cast(u32, t);
      }
      float lt = (bfhi(pk[0]) + bfhi(pk[1])) + (bfhi(pk[2]) + bfhi(pk[3]));
      lt += rorf<4>(lt); lt += rorf<8>(lt);
      lt = red16(lt, lt); lt = red32(lt, lt);
      if ((l & 61) == 0) { sm.wmax[l >> 1][w - 8] = mw; sm.wl[l >> 1][w - 8] = lt; }
      LDSWAIT();
      BUMP(cm);
    }
    if (w == 8) STAMP(9);
    WST(5);
    FENCE();
    const u32 hm = (l & 1) ? 0xffff0000u : 0x0000ffffu;
    const u32 m0 = bank == 0 ? hm : 0u, m1 = bank == 1 ? hm : 0u, m2 = bank == 2 ? hm : 0u, m3 = bank == 3 ? hm : 0u;
    u32x4 pb[16];
#define PBSET(t) do { pb[t][0] = roru<4 * (0 - ((t) & 3))>(pk[(t) >> 2]) & m0; pb[t][1] = roru<4 * (1 - ((t) & 3))>(pk[(t) >> 2]) & m1;   \
                      pb[t][2] = roru<4 * (2 - ((t) & 3))>(pk[(t) >> 2]) & m2; pb[t][3] = roru<4 * (3 - ((t) & 3))>(pk[(t) >> 2]) & m3;   \
                      asm volatile("" : "+v"(pb[t])); } while (0)
    f32x4 acc = {0.f, 0.f, 0.f, 0.f};
#define PV(i) do { acc = mfma(vv[i], pb[i], acc); asm volatile("" : "+v"(acc)); FENCE(); } while (0)
    if (QPOS < 6) VLD(8);
    if (QPOS < 7) VLD(9);
    PBSET(0); PBSET(1); PBSET(2); PBSET(3); PV(0); PV(1); PV(2); PV(3);
    VLD(10);
    PBSET(4); PBSET(5); PBSET(6); PBSET(7); PV(4);
    VLD(11);
    PV(5); PBSET(8); PBSET(9); PBSET(10); PBSET(11); FENCE();
    VLD(12);
    PV(6); PBSET(12); PBSET(13); PBSET(14); PBSET(15); FENCE();
    VLD(13);
    PV(7);
    VLD(14);
    PV(8);
    VLD(15);
    PV(9);
    float fsc;
    {
      LWAIT(cm, 8u);
      const float mi = sm.wmax[(l >> 1) & 1][(l >> 2) & 7];
      float M = fmaxf(mi, rorf<4>(mi)); M = fmaxf(M, rorf<8>(M)); M = max16(M);
      fsc = __builtin_amdgcn_exp2f(mw - M);
      asm volatile("" : "+v"(fsc));
      FENCE();
    }
    PV(10); PV(11); PV(12); PV(13); PV(14); PV(15);
    {
      float* ap = &sm.acc[w - 8][(((l >> 1) & 1) * 8 + 2 * (l >> 4)) * 16 + (bank * 2 + (l & 1)) * 2];
      *(f32x2*)ap = f32x2{acc[0] * fsc, acc[1] * fsc};
      *(f32x2*)(ap + 16) = f32x2{acc[2] * fsc, acc[3] * fsc};
      if (w == 8) STAMP(11);
      if (w == 15) STAMP(25);
      WST(6);
      if (arrive(&sm.ca, l) == 7u) {
        f32x4 t[8];
#pragma unroll
        for (int i = 0; i < 8; ++i) t[i] = *(const f32x4*)&sm.acc[i][4 * l];
        const f32x4 t0 = ((t[0] + t[1]) + (t[2] + t[3])) + ((t[4] + t[5]) + (t[6] + t[7]));
        const u32x4 r = {nosent(t0[0]), nosent(t0[1]), nosent(t0[2]), nosent(t0[3])};
        __builtin_amdgcn_raw_buffer_store_b128(r, rWS, WS_OX + (u32)(g * 16 + (l >> 2)) * (16 * OXS) + j * OXS + (l & 3) * 16, 0, AUX_SC1);
        STAMP(12);
      }
      FENCE();
    }
  }
  FENCE();
  if (w == 8) DWLOADS();
  FENCE();
  float y[8];
  if (ow) {
    const int rs = l >> 3;
    u32 h0, h1, h2;
    for (u32 it = 0;; ++it) {
      h0 = lds_ld(&sm.hv[rs]); h1 = lds_ld(&sm.hv[8 + rs]); h2 = lds_ld(&sm.hv[16 + rs]);
      if (__all(h0 != SENT && h1 != SENT && h2 != SENT) || it >= SPIN_L) break;
      __builtin_amdgcn_s_sleep(1);
    }
#pragma unroll
    for (int e = 0; e < 4; ++e) {
      y[2 * e] = __uint_as_float(h0) * bflo(dw[0][e]) + __uint_as_float(h1) * bflo(dw[1][e]) + __uint_as_float(h2) * bflo(dw[2][e]);
      y[2 * e + 1] = __uint_as_float(h0) * bfhi(dw[0][e]) + __uint_as_float(h1) * bfhi(dw[1][e]) + __uint_as_float(h2) * bfhi(dw[2][e]);
    }
    if (w == 0) STAMP(13);
  }
  FENCE();
  if (!ow) {
    const int p0 = w - 9;
    u32 wb_;
    for (u32 it = 0;; ++it) {
      wb_ = lds_ld(&sm.wgt[(p0 + 7 * (l >> 4)) & 15]);
      if (__all(wb_ != SENT) || it >= SPIN_L) break;
      __builtin_amdgcn_s_sleep(1);
    }
    const unsigned char* op = ws + WS_OX + (u32)u * (16 * OXS) + (u32)p0 * OXS;
    u32 pend = p0 < 2 ? 7u : 3u;
    for (u32 it = 0; it < SPIN_G && pend; ++it) {
      u32x16 v[3];
      if (pend & 1u) asm volatile("s_load_dwordx16 %0, %1, 0x0 glc" : "=&s"(v[0]) : "s"(op) : "memory");
      if (pend & 2u) asm volatile("s_load_dwordx16 %0, %1, %2 glc" : "=&s"(v[1]) : "s"(op), "n"(7 * OXS) : "memory");
      if (pend & 4u) asm volatile("s_load_dwordx16 %0, %1, %2 glc" : "=&s"(v[2]) : "s"(op), "n"(14 * OXS) : "memory");
      asm volatile("s_waitcnt lgkmcnt(0)" ::: "memory");
#pragma unroll
      for (int k = 0; k < 3; ++k) {
        if (pend & (1u << k)) {
          asm volatile("" : "+s"(v[k]));
          u32 mx = v[k][0];
#pragma unroll
          for (int e = 1; e < 16; ++e) mx = mx > v[k][e] ? mx : v[k][e];
          if (mx != SENT) {
            pend &= ~(1u << k);
            u32 t = 0;
#pragma unroll
            for (int e = 0; e < 16; ++e) asm("v_writelane_b32 %0, %1, %2" : "+v"(t) : "s"(v[k][e]), "n"(16 * k + e));
            const u32 r = nosent(__uint_as_float(t) * __uint_as_float(wb_));
            const int p = p0 + 7 * k;
            if ((l >> 4) == k) lds_st(&sm.op[(((l >> 1) & 7) * 8 + (p >> 1)) * 4 + (l & 1) * 2 + (p & 1)], r);
          }
        }
      }
    }
    if (pend) { lds_st(&sm.op[4 * l], 0u); lds_st(&sm.op[4 * l + 1], 0u); lds_st(&sm.op[4 * l + 2], 0u); lds_st(&sm.op[4 * l + 3], 0u); }
    if (w == 9) STAMP(20);
    if (w == 13) STAMP(15);
  }
  if (ow) {
    float wf[16];
#pragma unroll
    for (int e = 0; e < 4; ++e) { wf[2 * e] = bflo(dw[3][e]); wf[2 * e + 1] = bfhi(dw[3][e]); wf[8 + 2 * e] = bflo(dw[4][e]); wf[9 + 2 * e] = bfhi(dw[4][e]); }
    if (w == 0) STAMP(21);
    u32x4 o4;
    for (u32 it = 0;; ++it) {
      o4 = lds_ld4(&sm.op[4 * l]);
      if (__all(o4[0] != SENT && o4[1] != SENT && o4[2] != SENT && o4[3] != SENT) || it >= SPIN_L) break;
    }
    float o0 = __uint_as_float(o4[0]) + __uint_as_float(o4[1]), o1 = __uint_as_float(o4[2]) + __uint_as_float(o4[3]);
    o0 += dpp<0xB1>(o0); o1 += dpp<0xB1>(o1);
    o0 += dpp<0x4E>(o0); o1 += dpp<0x4E>(o1);
    o0 += dpp<0x141>(o0); o1 += dpp<0x141>(o1);
#pragma unroll
    for (int e = 0; e < 8; ++e) y[e] += o0 * wf[e] + o1 * wf[8 + e];
    float a4[4], a2[2];
#pragma unroll
    for (int e = 0; e < 4; ++e) a4[e] = red32(y[e], y[e + 4]);
#pragma unroll
    for (int e = 0; e < 2; ++e) a2[e] = red16(a4[e], a4[e + 2]);
    const float t_ = (l & 8) ? a2[0] : a2[1], kp_ = (l & 8) ? a2[1] : a2[0];
    const float a1 = kp_ + dpp<0x128>(t_);
    __builtin_amdgcn_raw_buffer_store_b32(nosent(a1), rWS, WS_FX + (u32)(4 * w + ((l >> 1) & 3)) * 16384u + (u32)u * 64u +
                                          (8 * (l & 1) + 4 * (l >> 5) + 2 * ((l >> 4) & 1) + ((l >> 3) & 1)) * 4, 0, AUX_SC1);
    if (w == 0) STAMP(14);
  }
  if (fcons) {
    if (w >= 9) {
      for (u32 it = 0; it < SPIN_L; ++it) {
        const u32x4 o4 = lds_ld4(&sm.op[4 * l]);
        if (__all(o4[0] != SENT && o4[1] != SENT && o4[2] != SENT && o4[3] != SENT)) break;
        __builtin_amdgcn_s_sleep(2);
      }
    }
#define FDELAY 4
    __builtin_amdgcn_s_sleep(FDELAY);
    const int col = 4 * (l & 3) + 2 * (l >> 5) + ((l >> 4) & 1);
    const u32 xo = *((lds_u16*)sm.xs + cidx * 16 + col);
    u32 fa = WS_FX + cidx * 16384u + (u32)w * 1024u + l * 16;
    u32x4 t = {0u, 0u, 0u, 0u};
    for (u32 it = 0; it < SPIN_G; ++it) {
      asm volatile("" ::: "memory");
      const u32x4 r = __builtin_amdgcn_raw_buffer_load_b128(rWS, fa, 0, AUX_SC1);
      if (fa != OOR && r[0] != SENT && r[1] != SENT && r[2] != SENT && r[3] != SENT) { t = r; fa = OOR; }
      if (__all(fa == OOR)) break;
    }
#define FRED(t) ({ float c_ = red16(red32(__uint_as_float(t[0]), __uint_as_float(t[2])), red32(__uint_as_float(t[1]), __uint_as_float(t[3]))); \
      c_ += dpp<0x124>(c_); c_ + dpp<0x128>(c_); })
    {
      const float c = FRED(t);
      if ((l & 12) == 0) lds_st(&sm.fin[w][col], nosent(c));
    }
    if (w == 0) STAMP(22);
    u32x4 f0;
    for (u32 it = 0;; ++it) {
      f0 = lds_ld4(&sm.fin[0][4 * l]);
      if (__all(f0[0] != SENT && f0[1] != SENT && f0[2] != SENT && f0[3] != SENT) || it >= SPIN_L) break;
      __builtin_amdgcn_s_sleep(1);
    }
    const float c = FRED(f0);
    const __bf16 r = (__bf16)(bflo(xo) + c);
    if ((l & 12) == 0) out[cidx * 16 + col] = __builtin_bit_cast(unsigned short, r);
    if (cidx == 0 && l == 0) __builtin_amdgcn_raw_buffer_store_b32(sm.ev + 1u, rWS, WS_E, 0, AUX_SC1);
    if (w == 0) STAMP(23);
  }
}

extern "C" void kernel_launch(void* const* d_in, const int* in_sizes, int n_in, void* d_out, int out_size, void* d_ws, size_t ws_size,
                              hipStream_t stream) {
  fused_decode<<<256, 1024, 0, stream>>>((const unsigned short*)d_in[0], (const unsigned short*)d_in[1], (const unsigned short*)d_in[2],
                                         (const unsigned short*)d_in[3], (const unsigned short*)d_in[4], (const unsigned short*)d_in[6],
                                         (const unsigned short*)d_in[7], (const unsigned short*)d_in[8], (unsigned char*)d_ws,
                                         (unsigned short*)d_out);
}
```

```cpp
#include <hip/hip_runtime.h>
#include <hip/hip_bf16.h>
#include <cstdint>

typedef unsigned u32;
typedef u32 u32x4 __attribute__((ext_vector_type(4)));
typedef u32 u32x2 __attribute__((ext_vector_type(2)));
typedef u32 u32x16 __attribute__((ext_vector_type(16)));
typedef float f32x4 __attribute__((ext_vector_type(4)));
typedef float f32x2 __attribute__((ext_vector_type(2)));
typedef __bf16 bf16x2 __attribute__((ext_vector_type(2)));
typedef __bf16 bf16x8 __attribute__((ext_vector_type(8)));
typedef __amdgpu_buffer_rsrc_t rsrc_t;

#define WIN 3

constexpr u32 WS_E = 0;
constexpr u32 WS_QX = 4096;
constexpr u32 WS_HX = WS_QX + 256 * 512;
constexpr u32 WS_FL = WS_HX + 256 * 1024;
#define OXS 64
constexpr u32 WS_AG = WS_FL + 256 * 128;
constexpr u32 WS_ML = WS_AG + 16 * 512;
constexpr u32 WS_OX = WS_ML + 256 * 128;
constexpr u32 WS_FX = WS_OX + 256 * 16 * OXS;
#define XMARK 0
constexpr u32 WS_END = WS_FX + (36 + XMARK) * 16384;
constexpr u32 SPIN_G = 40000;
constexpr u32 SPIN_L = 4000000;
constexpr u32 SENT = 0xFFFFFFFFu;
constexpr u32 OOR = 0xFFF00000u;

#define STAMP(i) do { } while (0)
#define WST(id) do { } while (0)

#define AUX_NT 2
#define AUX_SC1 16
#define FENCE() __builtin_amdgcn_sched_barrier(0)
#define THR(n) do { asm volatile("s_waitcnt vmcnt(%0)" :: "n"(n) : "memory"); FENCE(); } while (0)
#define THROTTLE() THR(WIN - 1)
#define LDSWAIT() asm volatile("s_waitcnt lgkmcnt(0)" ::: "memory")

__device__ __forceinline__ float bflo(u32 u) { return __uint_as_float(u << 16); }
__device__ __forceinline__ float bfhi(u32 u) { return __uint_as_float(u & 0xffff0000u); }
__device__ __forceinline__ float rbf(float a) { return bflo(__builtin_bit_cast(unsigned short, (__bf16)a)); }
template <int CTRL> __device__ __forceinline__ float dpp(float x) {
  return __int_as_float(__builtin_amdgcn_update_dpp(0, __float_as_int(x), CTRL, 0xf, 0xf, true));
}
template <int N> __device__ __forceinline__ float rorf(float x) {
  if constexpr ((N & 15) == 0) return x; else return dpp<0x120 + (N & 15)>(x);
}
template <int N> __device__ __forceinline__ u32 roru(u32 x) {
  if constexpr ((N & 15) == 0) return x; else return (u32)__builtin_amdgcn_update_dpp(0, (int)x, 0x120 + (N & 15), 0xf, 0xf, true);
}
__device__ __forceinline__ float red32(float a, float b) {
  auto r = __builtin_amdgcn_permlane32_swap(__float_as_uint(a), __float_as_uint(b), false, false);
  return __uint_as_float(r[0]) + __uint_as_float(r[1]);
}
__device__ __forceinline__ float red16(float a, float b) {
  auto r = __builtin_amdgcn_permlane16_swap(__float_as_uint(a), __float_as_uint(b), false, false);
  return __uint_as_float(r[0]) + __uint_as_float(r[1]);
}
__device__ __forceinline__ float max32(float a) {
  auto r = __builtin_amdgcn_permlane32_swap(__float_as_uint(a), __float_as_uint(a), false, false);
  return fmaxf(__uint_as_float(r[0]), __uint_as_float(r[1]));
}
__device__ __forceinline__ float max16(float a) {
  auto r = __builtin_amdgcn_permlane16_swap(__float_as_uint(a), __float_as_uint(a), false, false);
  return fmaxf(__uint_as_float(r[0]), __uint_as_float(r[1]));
}
__device__ __forceinline__ float rowsum16(float s) {
  s += dpp<0xB1>(s); s += dpp<0x4E>(s); s += dpp<0x141>(s); s += dpp<0x140>(s);
  return s;
}
__device__ __forceinline__ float rowmax16(float s) {
  s = fmaxf(s, dpp<0xB1>(s)); s = fmaxf(s, dpp<0x4E>(s)); s = fmaxf(s, dpp<0x141>(s)); s = fmaxf(s, dpp<0x140>(s));
  return s;
}
__device__ __forceinline__ float wavesum(float s) { s = rowsum16(s); s = red16(s, s); s = red32(s, s); return s; }
__device__ __forceinline__ rsrc_t mkr(const void* p, u32 n) { return __builtin_amdgcn_make_buffer_rsrc((void*)p, 0, n, 0x00020000); }
__device__ __forceinline__ u32x4 gran(float a, float b, u32 tag) { u32x4 r = {__float_as_uint(a), tag, __float_as_uint(b), tag}; return r; }
__device__ __forceinline__ bool tagok(u32x4 v, u32 tag) { return v[1] == tag && v[3] == tag; }
typedef __attribute__((address_space(3))) volatile u32 lds_u32;
typedef __attribute__((address_space(3))) volatile u32x4 lds_u32x4;
typedef __attribute__((address_space(3))) volatile unsigned short lds_u16;
__device__ __forceinline__ u32 lds_ld(u32* p) { return *(lds_u32*)p; }
__device__ __forceinline__ u32x4 lds_ld4(u32* p) { return *(lds_u32x4*)p; }
__device__ __forceinline__ void lds_st(u32* p, u32 v) { *(lds_u32*)p = v; }
#define PIN8(a) asm volatile("" : "+v"(a[0]), "+v"(a[1]), "+v"(a[2]), "+v"(a[3]), "+v"(a[4]), "+v"(a[5]), "+v"(a[6]), "+v"(a[7]))
__device__ __forceinline__ u32 nosent(float v) { return min(__float_as_uint(v), SENT - 1u); }
__device__ __forceinline__ u32 arrive(u32* c, int l) {
  u32 old = 0;
  LDSWAIT();
  if (l == 0) old = __hip_atomic_fetch_add(c, 1u, __ATOMIC_RELAXED, __HIP_MEMORY_SCOPE_WORKGROUP);
  old = __builtin_amdgcn_readfirstlane(old);
  asm volatile("" ::: "memory");
  return old;
}
__device__ __forceinline__ f32x4 mfma(u32x4 a, u32x4 b, f32x4 c) {
  return __builtin_amdgcn_mfma_f32_16x16x32_bf16(__builtin_bit_cast(bf16x8, a), __builtin_bit_cast(bf16x8, b), c, 0, 0, 0);
}

struct __align__(16) Smem {
  float qp[8][64];
  float hs[12][64];
  float acc[8][256];
  float wmax[2][8];
  float wl[2][8];
  u32 qv[2][2][64];
  u32 xs[320];
  float xn[640];
  u32 op[256];
  u32 fin[16][16];
  u32 hv[24];
  u32 wgt[16];
  u32 cq, ch, cm, ca, fq, fx, cv, ff;
  u32 an, pad3, pad4, ev;
};
#define WA 3
#define NA 3
#define WB 8
#define NB 32
#define WC 6
#define NC 32
#define WD 6
#define BARI 2
#define TRIGN 8u
#define QSLP 4
#define HSLP 8
#define HSLP0 32
#define AUX_1ST 0
#define MSLP 4
#define MSLP0 16
#define AUX_E 0
#define WS_ 3
#define QPOS 7
#define TRIG 15
#define PMODE 0
#define PIT 4
#define PTH 1
#define PACE_AT(base, T) do {                                                                                               \
      asm volatile("1: s_memrealtime s[94:95]\n s_waitcnt lgkmcnt(0)\n s_sub_u32 s94, s94, %0\n s_cmpk_lt_i32 s94, %1\n"    \
                   " s_cbranch_scc0 2f\n s_sleep 1\n s_branch 1b\n2:"                                                      \
                   :: "s"(base), "n"(T) : "s94", "s95", "scc", "memory");                                                   \
      FENCE(); } while (0)
#define PN0 3
#define PT0 2050
#define N1 4
#define N2 17
#define TAU1 285
#define TAU2 400
#define TAU3 285
constexpr int TK(int n) { return n <= N1 ? PT0 + (n - PN0) * TAU1 : n <= N2 ? TK(N1) + (n - N1) * TAU2 : TK(N2) + (n - N2) * TAU3; }
#define GT0 2600
#define GTAU 800
#define DOFF 700
#define DTAU 400
#define DSTAG 0
#define GSL 0
#define AOFF 0
#define ACL 100
#define ANCHOR(dst) do { const u32 a_ = lds_ld(&sm.an);                                                                     \
      dst = __builtin_amdgcn_readfirstlane(a_ ? tb + (u32)min(max((int)(a_ - tb), -ACL), ACL) : tb); FENCE(); } while (0)
#define BUMP(c) do { if (l == 0) __hip_atomic_fetch_add(&sm.c, 1u, __ATOMIC_RELAXED, __HIP_MEMORY_SCOPE_WORKGROUP); } while (0)
#define LWAIT(c, n) do { for (u32 it = 0; lds_ld(&sm.c) != (n) && it < SPIN_L; ++it) __builtin_amdgcn_s_sleep(1); asm volatile("" ::: "memory"); } while (0)

__global__ void __launch_bounds__(1024, 4)
fused_decode(const unsigned short* __restrict__ x, const unsigned short* __restrict__ Kc, const unsigned short* __restrict__ Vc,
             const unsigned short* __restrict__ wq, const unsigned short* __restrict__ wo, const unsigned short* __restrict__ wg,
             const unsigned short* __restrict__ wu, const unsigned short* __restrict__ wd, unsigned char* __restrict__ ws,
             unsigned short* __restrict__ out) {
  __shared__ Smem sm;
  const unsigned long long tb64 = __builtin_amdgcn_s_memrealtime();
#define CPF 1
#define CODEB 16896u
#define CPS 8u
  u32 cpf;
  {
    unsigned long long pc, sv;
    const u32 po = ((1023u - threadIdx.x) * CPS + ((blockIdx.x >> 3) & (CPS - 1u))) * 128u;
    asm volatile("s_getpc_b64 %1\n v_cmp_ge_u32 vcc, %4, %3\n s_and_saveexec_b64 %2, vcc\n s_cbranch_execz 1f\n"
                 " global_load_dword %0, %3, %1\n1: s_mov_b64 exec, %2"
                 : "=&v"(cpf), "=&s"(pc), "=&s"(sv) : "v"(po), "s"(CODEB) : "vcc", "scc", "memory");
  }
  const int b = blockIdx.x;
  const int w = __builtin_amdgcn_readfirstlane(threadIdx.x >> 6);
  const int l = threadIdx.x & 63;
#define PXA 0
#define PXB 0
  const int u = ((b + PXA) & 7) * 32 + ((b >> 3) ^ PXB);
  const int g = u >> 4, j = u & 15, k4 = u & 3;
  const bool fcons = b < 36;
  const u32 cidx = (u32)b;
#define LB0 8
#define LB1 9
#define WB0 14
#define WB1 15
#define WB2 16
#define TB0 10
#define TB1 11
#define TB2 12
#define TB3 13
#define TOF(i) ((((i) & 1) << TB0) | ((((i) >> 1) & 1) << TB1) | ((((i) >> 2) & 1) << TB2) | ((((i) >> 3) & 1) << TB3))
#define TVO(i) (TOF(i) & 4095)
#define TSO(i) (TOF(i) & ~4095)
  const u32 kvb = (u32)u * 131072u + (u32)(((w & 1) << WB0) | (((w >> 1) & 1) << WB1) | (((w >> 2) & 1) << WB2));
  const int rb = 144 * k4 + 18 * w;
  u32 fo0 = w < 8 ? (u32)(rb + (l >> 3)) * 8192u + (u32)(256 * g + 64 * (j >> 2)) * 2 + (l & 7) * 16
                  : kvb + (u32)((((l >> 2) & 1) << LB0) | (((l >> 3) & 1) << LB1) | ((l >> 4) * 64 + (l & 3) * 16));
  u32 fo2 = (w < 8 && l >= 16) ? OOR : fo0;
  u32 fs1 = w < 8 ? 8u * 8192u : (u32)TOF(1), fs2 = w < 8 ? 16u * 8192u : (u32)TOF(2), fsz = w < 8 ? 576u * 8192u : 33554432u;
  u32 xo0 = l * 16, xo1 = 1024 + l * 4;
  asm volatile("" : "+v"(fo0), "+v"(fo2), "+s"(fs1), "+s"(fs2), "+s"(fsz), "+v"(xo0), "+v"(xo1));
  FENCE();
  asm volatile("" :: "s"(x), "s"(Kc), "s"(Vc), "s"(wq), "s"(wo), "s"(wg), "s"(wu), "s"(wd), "s"(ws), "s"(tb64));
  const u32 tb = (u32)tb64;
#define TP(id) do { } while (0)
#define TPW() do { } while (0)
  TP(7);
  const rsrc_t rWS = mkr(ws, WS_END);
  u32x4 xa = {0u, 0u, 0u, 0u}, f1[3];
  u32 xb = 0u, Ev = 0u;
  if (w == 0) {
    const rsrc_t rX = mkr(x, 1152);
    xa = __builtin_amdgcn_raw_buffer_load_b128(rX, xo0, 0, 0);
    xb = __builtin_amdgcn_raw_buffer_load_b32(rX, xo1, 0, 0);
    Ev = __builtin_amdgcn_raw_buffer_load_b32(rWS, WS_E, 0, AUX_E);
  }
  FENCE();
  {
    const rsrc_t r1 = mkr(w < 8 ? wq : Kc, fsz);
    f1[0] = __builtin_amdgcn_raw_buffer_load_b128(r1, fo0, 0, AUX_NT); FENCE();
    TP(8);
    f1[1] = __builtin_amdgcn_raw_buffer_load_b128(r1, fo0, fs1, AUX_NT); FENCE();
    f1[2] = __builtin_amdgcn_raw_buffer_load_b128(r1, fo2, fs2, AUX_NT); FENCE();
  }
  if (w < 8) {
    const u32x4 sv = {SENT, SENT, SENT, SENT};
    if (w == 7) __builtin_amdgcn_raw_buffer_store_b128(sv, rWS, WS_OX + (u32)u * (16 * OXS) + (l >> 2) * OXS + (l & 3) * 16, 0, AUX_SC1);
    if (w == 6) __builtin_amdgcn_raw_buffer_store_b128(sv, rWS, l < 8 ? WS_ML + (u32)u * 128u + l * 16 : OOR, 0, AUX_SC1);
    FENCE();
    if (b < 36 + XMARK) {
#pragma unroll
      for (int i = 0; i < 2; ++i) __builtin_amdgcn_raw_buffer_store_b128(sv, rWS, WS_FX + cidx * 16384u + (u32)w * 2048u + l * 16, i * 1024, AUX_SC1);
    }
    FENCE();
    if (threadIdx.x < 281) {
      const u32 iv = threadIdx.x < 276 ? SENT : 0u;
      *(u32x2*)&sm.op[2 * threadIdx.x] = u32x2{iv, iv};
    }
  }
  FENCE();
  TP(9);
  LDSWAIT(); __builtin_amdgcn_s_barrier(); FENCE(); WST(0); TP(10);
  TPW();
  const bool ow = w < 9;
  const rsrc_t rD = mkr(wd, 4608u * 1152u), rO = mkr(wo, 4096u * 1152u);
  u32 od, oo;
#define DWADDR() do { int l_ = l; asm volatile("" : "+v"(l_)); const u32 co = (u32)w * 128u + (l_ & 7) * 16;      \
    od = (u32)(18 * u + (l_ >> 3)) * 1152u + co; oo = (u32)(16 * u + (l_ >> 3)) * 1152u + co; } while (0)
  u32x4 dw[5];
#define DWLOADS() do {                                                                                     \
    dw[0] = __builtin_amdgcn_raw_buffer_load_b128(rD, od, 0, AUX_NT);                                      \
    dw[1] = __builtin_amdgcn_raw_buffer_load_b128(rD, od, 8 * 1152, AUX_NT);                               \
    dw[2] = __builtin_amdgcn_raw_buffer_load_b128(rD, l < 16 ? od : OOR, 16 * 1152, AUX_NT);               \
    dw[3] = __builtin_amdgcn_raw_buffer_load_b128(rO, oo, 0, AUX_NT);                                      \
    dw[4] = __builtin_amdgcn_raw_buffer_load_b128(rO, oo, 8 * 1152, AUX_NT); FENCE(); } while (0)

  if (w >= 8) {
    const rsrc_t rK = mkr(Kc, 33554432u), rV = mkr(Vc, 33554432u);
    u32x4 kk[16], vv[16];
#define WINOF(n) ((n) < NA ? WA : (n) < NB ? WB : (n) < NC ? WC : WD)
    u32 tk = tb;
#define PACE(n) do { if ((n) >= PN0) PACE_AT(tk, TK(n) / 10); } while (0)
#define KLD(i) do { if ((i) >= WA) THR(WINOF(i) - 1);                                                                      \
      PACE(i);                                                                                                              \
      kk[i] = __builtin_amdgcn_raw_buffer_load_b128(rK, koff + TVO(i), TSO(i), AUX_NT); FENCE(); } while (0)
#define VLD(i) do { if ((i) % 3 == 1 && (i) < 12) ANCHOR(tk); THR(WINOF(16 + (i)) - 1);                                                                               \
      PACE(16 + (i));                                                                                                       \
      vv[i] = __builtin_amdgcn_raw_buffer_load_b128(rV, voff + TVO(i), TSO(i), AUX_NT); FENCE();                            \
      if ((i) == TRIG) { BUMP(cv); FENCE(); } } while (0)
    const u32 koff = fo0;
    kk[0] = f1[0]; kk[1] = f1[1]; kk[2] = f1[2];
    KLD(3); KLD(4); KLD(5); KLD(6); KLD(7);
    asm volatile("" :: "v"(cpf));
    WST(1);
    KLD(8); KLD(9); KLD(10); KLD(11); KLD(12); KLD(13); KLD(14);
    int lv = l;
    asm volatile("" : "+v"(lv));
    const u32 voff = kvb + (u32)((((lv >> 4) & 1) << LB0) | ((lv >> 5) << LB1) | ((lv & 15) * 16));
    KLD(15);
    WST(2);
    VLD(0); VLD(1); VLD(2); VLD(3); VLD(4); VLD(5);
    if (QPOS >= 6) VLD(6);
    if (QPOS >= 7) VLD(7);
    WST(3);
    LWAIT(fq, 2u);
    if (w == 8) STAMP(8);
    WST(4);
    const int bank = (l >> 2) & 3;
    u32 pk[4];
    float mw;
    {
      const u32x4 qb = *(const u32x4*)&sm.qv[(l >> 1) & 1][l & 1][4 * ((l >> 4) * 4 + bank)];
      float sc[4];
#pragma unroll
      for (int ab = 0; ab < 4; ++ab) {
        const f32x4 z = {0.f, 0.f, 0.f, 0.f};
        const f32x4 d0 = mfma(kk[4 * ab], qb, z), d1 = mfma(kk[4 * ab + 1], qb, z), d2 = mfma(kk[4 * ab + 2], qb, z), d3 = mfma(kk[4 * ab + 3], qb, z);
        float a0 = ((d0[0] + rorf<-4>(d0[1])) + rorf<-8>(d0[2])) + rorf<-12>(d0[3]);
        float a1 = ((d1[1] + rorf<4>(d1[0])) + rorf<-4>(d1[2])) + rorf<-8>(d1[3]);
        float a2 = ((d2[2] + rorf<8>(d2[0])) + rorf<4>(d2[1])) + rorf<-4>(d2[3]);
        float a3 = ((d3[3] + rorf<12>(d3[0])) + rorf<8>(d3[1])) + rorf<4>(d3[2]);
        asm volatile("" : "+v"(a0), "+v"(a1), "+v"(a2), "+v"(a3));
        const float t01 = (l & 4) ? a1 : a0, t23 = (l & 4) ? a3 : a2;
        const float t = (l & 8) ? t23 : t01;
        sc[ab] = rbf(rbf(t + dpp<0xB1>(t)) * 0.08838834764831845f) * 1.4426950408889634f;
        asm volatile("" : "+v"(sc[ab]));
        if (ab == 1) { FENCE(); VLD(QPOS + 1); }
      }
      FENCE(); VLD(QPOS + 2);
      float m = fmaxf(fmaxf(sc[0], sc[1]), fmaxf(sc[2], sc[3]));
      m = fmaxf(m, rorf<4>(m)); m = fmaxf(m, rorf<8>(m));
      m = max32(max16(m));
      mw = m;
#pragma unroll
      for (int ab = 0; ab < 4; ++ab) {
        const float p = __builtin_amdgcn_exp2f(sc[ab] - m);
        const bf16x2 t = {(__bf16)p, (__bf16)p};
        pk[ab] = __builtin_bit_cast(u32, t);
      }
      float lt = (bfhi(pk[0]) + bfhi(pk[1])) + (bfhi(pk[2]) + bfhi(pk[3]));
      lt += rorf<4>(lt); lt += rorf<8>(lt);
      lt = red16(lt, lt); lt = red32(lt, lt);
      if ((l & 61) == 0) { sm.wmax[l >> 1][w - 8] = mw; sm.wl[l >> 1][w - 8] = lt; }
      LDSWAIT();
      BUMP(cm);
    }
    if (w == 8) STAMP(9);
    WST(5);
    FENCE();
    const u32 hm = (l & 1) ? 0xffff0000u : 0x0000ffffu;
    const u32 m0 = bank == 0 ? hm : 0u, m1 = bank == 1 ? hm : 0u, m2 = bank == 2 ? hm : 0u, m3 = bank == 3 ? hm : 0u;
    u32x4 pb[16];
#define PBSET(t) do { pb[t][0] = roru<4 * (0 - ((t) & 3))>(pk[(t) >> 2]) & m0; pb[t][1] = roru<4 * (1 - ((t) & 3))>(pk[(t) >> 2]) & m1;   \
                      pb[t][2] = roru<4 * (2 - ((t) & 3))>(pk[(t) >> 2]) & m2; pb[t][3] = roru<4 * (3 - ((t) & 3))>(pk[(t) >> 2]) & m3;   \
                      asm volatile("" : "+v"(pb[t])); } while (0)
    f32x4 acc = {0.f, 0.f, 0.f, 0.f};
#define PV(i) do { acc = mfma(vv[i], pb[i], acc); asm volatile("" : "+v"(acc)); FENCE(); } while (0)
    if (QPOS < 6) VLD(8);
    if (QPOS < 7) VLD(9);
    PBSET(0); PBSET(1); PBSET(2); PBSET(3); PV(0); PV(1); PV(2); PV(3);
    VLD(10);
    PBSET(4); PBSET(5); PBSET(6); PBSET(7); PV(4);
    VLD(11);
    PV(5); PBSET(8); PBSET(9); PBSET(10); PBSET(11); FENCE();
    VLD(12);
    PV(6); PBSET(12); PBSET(13); PBSET(14); PBSET(15); FENCE();
    VLD(13);
    PV(7);
    VLD(14);
    PV(8);
    VLD(15);
#define GPRIO 0
    __builtin_amdgcn_s_setprio(GPRIO);
#define W8P 16
#define FASTA 1
    u32 apo = (u32)(size_t)(lds_u32*)(u32*)&sm.acc[w - 8][(((l >> 1) & 1) * 8 + 2 * (l >> 4)) * 16 + (bank * 2 + (l & 1)) * 2];
    u32 rao = (u32)(size_t)(lds_u32*)(u32*)&sm.acc[0][4 * l], cao = (u32)(size_t)(lds_u32*)&sm.ca, one = 1u;
    u32 oxo = WS_OX + (u32)(g * 16 + (l >> 2)) * (16 * OXS) + j * OXS + (l & 3) * 16;
    asm volatile("" : "+v"(apo), "+v"(rao), "+v"(cao), "+v"(one), "+v"(oxo));
    u32 o2 = OOR;
    if (W8P < 16) {
      DWADDR();
      od = w == 8 ? od : OOR; oo = w == 8 ? oo : OOR; o2 = l < 16 ? od : OOR;
      asm volatile("" : "+v"(od), "+v"(oo), "+v"(o2));
      FENCE();
    }
    PV(9);
    float fsc;
    {
      LWAIT(cm, 8u);
      const float mi = sm.wmax[(l >> 1) & 1][(l >> 2) & 7];
      float M = fmaxf(mi, rorf<4>(mi)); M = fmaxf(M, rorf<8>(M)); M = max16(M);
      fsc = __builtin_amdgcn_exp2f(mw - M);
      asm volatile("" : "+v"(fsc));
      FENCE();
    }
#define W8L(i) do { if (W8P == (i)) {                                                                      \
    dw[0] = __builtin_amdgcn_raw_buffer_load_b128(rD, od, 0, AUX_NT);                                      \
    dw[1] = __builtin_amdgcn_raw_buffer_load_b128(rD, od, 8 * 1152, AUX_NT);                               \
    dw[2] = __builtin_amdgcn_raw_buffer_load_b128(rD, o2, 16 * 1152, AUX_NT);                              \
    dw[3] = __builtin_amdgcn_raw_buffer_load_b128(rO, oo, 0, AUX_NT);                                      \
    dw[4] = __builtin_amdgcn_raw_buffer_load_b128(rO, oo, 8 * 1152, AUX_NT); FENCE(); } } while (0)
    PV(10); W8L(10); PV(11); W8L(11); PV(12); W8L(12); PV(13); W8L(13); PV(14); W8L(14); PV(15);
    {
      f32x4 t[8];
      u32 old;
      const f32x2 a0 = {acc[0] * fsc, acc[1] * fsc}, a1 = {acc[2] * fsc, acc[3] * fsc};
      asm volatile("ds_write2_b64 %[ap], %[a0], %[a1] offset1:8\n s_mov_b64 exec, 1\n ds_add_rtn_u32 %[old], %[ca], %[one]\n s_mov_b64 exec, -1\n"
                   " ds_read_b128 %[t0], %[ra]\n ds_read_b128 %[t1], %[ra] offset:1024\n ds_read_b128 %[t2], %[ra] offset:2048\n"
                   " ds_read_b128 %[t3], %[ra] offset:3072\n ds_read_b128 %[t4], %[ra] offset:4096\n ds_read_b128 %[t5], %[ra] offset:5120\n"
                   " ds_read_b128 %[t6], %[ra] offset:6144\n ds_read_b128 %[t7], %[ra] offset:7168\n s_waitcnt lgkmcnt(4)"
                   : [old] "=&v"(old), [t0] "=&v"(t[0]), [t1] "=&v"(t[1]), [t2] "=&v"(t[2]), [t3] "=&v"(t[3]), [t4] "=&v"(t[4]), [t5] "=&v"(t[5]),
                     [t6] "=&v"(t[6]), [t7] "=&v"(t[7])
                   : [ap] "v"(apo), [a0] "v"(a0), [a1] "v"(a1), [ca] "v"(cao), [one] "v"(one), [ra] "v"(rao) : "memory");
      FENCE();
      f32x4 s0 = (t[0] + t[1]) + (t[2] + t[3]);
      asm volatile("s_waitcnt lgkmcnt(0)" : "+v"(s0), "+v"(t[4]), "+v"(t[5]), "+v"(t[6]), "+v"(t[7]) :: "memory");
      const f32x4 t0 = s0 + ((t[4] + t[5]) + (t[6] + t[7]));
      const u32x4 r = {nosent(t0[0]), nosent(t0[1]), nosent(t0[2]), nosent(t0[3])};
      if (w == 8) STAMP(11);
      if (__builtin_amdgcn_readfirstlane(old) == 7u) {
        __builtin_amdgcn_raw_buffer_store_b128(r, rWS, oxo, 0, AUX_SC1);
        STAMP(12);
      }
      FENCE();
    }
  } else {
    const u32 ts0 = tb + (u32)(w * (GTAU / 60));
    const rsrc_t rG = mkr((g & 1) ? wu : wg, 576u * 9216u);
    const int uB = 9 * j + 3 * (w >> 1) + 1, uA = uB - 1 + 2 * (w & 1);
    const int rA = 36 * (uA & 15), rB = 36 * (uB & 15) + 18 * (w & 1);
    const u32 gv = w < 6 ? (u32)(l >> 3) * 9216u + (u32)(u >> 5) * 1152u + (l & 7) * 16 : OOR;
    const u32 gB = w < 6 ? (u32)(rB * 9216 + (uB >> 4) * 128) : 0u, gA = (u32)(rA * 9216 + (uA >> 4) * 128);
#define AUX_G AUX_NT
    u32x4 fg[7];
    fg[0] = __builtin_amdgcn_raw_buffer_load_b128(rG, gv, gB, AUX_G); FENCE();
    if (w == 0) {
      float xf[10];
#pragma unroll
      for (int e = 0; e < 4; ++e) { xf[2 * e] = bflo(xa[e]); xf[2 * e + 1] = bfhi(xa[e]); }
      xf[8] = bflo(xb); xf[9] = bfhi(xb);
      const float mean = wavesum((((xf[0] + xf[1]) + (xf[2] + xf[3])) + ((xf[4] + xf[5]) + (xf[6] + xf[7]))) + (xf[8] + xf[9])) * (1.0f / 576.0f);
      const float mb = rbf(mean);
      float xd[10];
#pragma unroll
      for (int e = 0; e < 10; ++e) { xd[e] = rbf(xf[e] - mb); xf[e] -= mean; }
      float v0 = xf[0] * xf[0], v1 = xf[1] * xf[1];
#pragma unroll
      for (int e = 2; e < 8; e += 2) { v0 += xf[e] * xf[e]; v1 += xf[e + 1] * xf[e + 1]; }
      v0 += l < 32 ? xf[8] * xf[8] + xf[9] * xf[9] : 0.f;
      const float rs = __builtin_amdgcn_rcpf(rbf(__builtin_amdgcn_sqrtf(rbf(rbf(wavesum(v0 + v1) * (1.0f / 576.0f)) + 1e-5f))));
#pragma unroll
      for (int e = 0; e < 10; ++e) xf[e] = rbf(xd[e] * rs);
      *(f32x4*)&sm.xn[8 * l] = f32x4{xf[0], xf[1], xf[2], xf[3]};
      *(f32x4*)&sm.xn[8 * l + 4] = f32x4{xf[4], xf[5], xf[6], xf[7]};
      *(f32x2*)&sm.xn[512 + 2 * l] = f32x2{xf[8], xf[9]};
      *(u32x4*)&sm.xs[4 * l] = xa;
      sm.xs[256 + l] = xb;
      if (l == 0) sm.ev = Ev;
      LDSWAIT();
      if (l == 0) lds_st(&sm.fx, 1u);
      STAMP(2);
    }
    FENCE();
    LWAIT(fx, 1u);
    const u32 tag = __builtin_amdgcn_readfirstlane(sm.ev) * 0x9E3779B1u + 0x7F4A7C15u;
    float xq[3], xg[8];
#pragma unroll
    for (int i = 0; i < 3; ++i) xq[i] = sm.xn[rb + (l >> 3) + 8 * i];
    asm volatile("" : "+v"(xq[0]), "+v"(xq[1]), "+v"(xq[2]));
    FENCE();
    THR(1);
    WST(1);
#define FACC(N, G, F, X) do {                                                                                          \
      _Pragma("unroll") for (int e = 0; e < 4; ++e) G[e] = f32x2{X[0], X[0]} * f32x2{bflo(F[0][e]), bfhi(F[0][e])};    \
      _Pragma("unroll") for (int i = 1; i < N; ++i)                                                                    \
        _Pragma("unroll") for (int e = 0; e < 4; ++e) G[e] += f32x2{X[i], X[i]} * f32x2{bflo(F[i][e]), bfhi(F[i][e])}; \
    } while (0)
    {
      f32x2 g2[4];
      FACC(3, g2, f1, xq);
      float a4[4], a2[2];
#pragma unroll
      for (int e = 0; e < 4; ++e) a4[e] = red32(g2[e >> 1][e & 1], g2[2 + (e >> 1)][e & 1]);
#pragma unroll
      for (int e = 0; e < 2; ++e) a2[e] = red16(a4[e], a4[e + 2]);
      const float t_ = (l & 8) ? a2[0] : a2[1], kp_ = (l & 8) ? a2[1] : a2[0];
      sm.qp[w][8 * (l & 7) + 4 * (l >> 5) + 2 * ((l >> 4) & 1) + ((l >> 3) & 1)] = kp_ + dpp<0x128>(t_);
      if (w == 0) STAMP(1);
      if (arrive(&sm.cq, l) == 7u) {
        const int P_ = l & 31, hf = l >> 5;
        const f32x2 s_ = (*(const f32x2*)&sm.qp[hf * 4][2 * P_] + *(const f32x2*)&sm.qp[hf * 4 + 1][2 * P_]) +
                         (*(const f32x2*)&sm.qp[hf * 4 + 2][2 * P_] + *(const f32x2*)&sm.qp[hf * 4 + 3][2 * P_]);
        const float s0 = red32(s_[0], s_[0]), s1 = red32(s_[1], s_[1]);
        if (l < 32) __builtin_amdgcn_raw_buffer_store_b128(gran(s0, s1, tag), rWS, WS_QX + (u32)u * 512u + P_ * 16, 0, AUX_SC1);
        __builtin_amdgcn_raw_buffer_store_b32(tag, rWS, l < 16 ? WS_FL + (u32)(g * 16 + l) * 128u + j * 4 : OOR, 0, AUX_SC1);
        if (fcons) __builtin_amdgcn_raw_buffer_store_b64(u32x2{tb, tag}, rWS, l < 16 ? WS_AG + (u32)l * 512u + cidx * 8u : OOR, 0, AUX_SC1);
        STAMP(3);
      }
      FENCE();
    }
    if (w < 6) {
      const float* const xr = &sm.xn[l >> 3];
      xg[0] = xr[rB]; xg[1] = xr[rB + 8];
      xg[2] = xr[l < 16 ? rB + 16 : rA - 2];
      xg[4] = xr[rA + 6]; xg[5] = xr[rA + 14]; xg[6] = xr[rA + 22]; xg[7] = xr[rA + 30];
      xg[7] = l < 48 ? xg[7] : 0.f;
      xg[3] = l < 16 ? 0.f : xg[2];
      xg[2] = l < 16 ? xg[2] : 0.f;
      const int c0 = (9 * j) >> 4, n0 = 16 - ((9 * j) & 15);
      const int s0 = j - ((c0 * 455) >> 8);
      const int c1 = n0 < 9 ? c0 + 1 : j == 0 ? 0 : j == 6 ? 4 : 8, s1 = n0 < 9 ? 0 : 2;
      const u32 cA = (u32)(64 * c0 + l), cB = (u32)(64 * c1 + l), qA = (cA * 3641u) >> 16, qB = (cB * 3641u) >> 16;
      const u32 hbase = WS_HX + (u32)(u & ~31) * 1024u + (u32)(g & 1) * 432u;
      u32 hA = hbase + (u32)s0 * 144u + qA * 1024u + (cA - 18u * qA) * 8u;
      u32 hB = (n0 < 9 || j == 0 || j == 6 || j == 15) ? hbase + (u32)s1 * 144u + qB * 1024u + (cB - 18u * qB) * 8u : OOR;
      u32 gv2 = gv + (l < 16 ? gB + 16u * 9216u : gA - 2u * 9216u), gv6 = l < 48 ? gv : OOR;
      PIN8(xg);
      asm volatile("" : "+v"(hA), "+v"(hB), "+v"(gv2), "+v"(gv6));
      const int cl = 8 * (l & 7) + 4 * (l >> 5) + 2 * ((l >> 4) & 1) + ((l >> 3) & 1);
#define GLD(k, vo, so) do { PACE_AT(ts0, (GT0 + ((k) - 1) * GTAU) / 10); fg[k] = __builtin_amdgcn_raw_buffer_load_b128(rG, vo, so, AUX_G); FENCE(); } while (0)
#define COLSUM(G) ({ float a4_[4], a2_[2];                                                                              \
      _Pragma("unroll") for (int e = 0; e < 4; ++e) a4_[e] = red32(G[e >> 1][e & 1], G[2 + (e >> 1)][e & 1]);           \
      _Pragma("unroll") for (int e = 0; e < 2; ++e) a2_[e] = red16(a4_[e], a4_[e + 2]);                                 \
      const float t_ = (l & 8) ? a2_[0] : a2_[1], kp_ = (l & 8) ? a2_[1] : a2_[0];                                      \
      kp_ + dpp<0x128>(t_); })
      GLD(1, gv, gB + 8u * 9216u); GLD(2, gv2, 0); GLD(3, gv, gA + 6u * 9216u); GLD(4, gv, gA + 14u * 9216u);
      {
        f32x2 g2[4];
        FACC(3, g2, fg, xg);
        sm.hs[2 * (w & 6) + 1 + (w & 1)][cl] = COLSUM(g2);
        FENCE();
      }
      GLD(5, gv, gA + 22u * 9216u); GLD(6, gv6, gA + 30u * 9216u);
      {
        f32x2 g2[4];
        FACC(5, g2, (fg + 2), (xg + 3));
        sm.hs[2 * (w & 6) + 3 * (w & 1)][cl] = COLSUM(g2);
        WST(2);
        if (arrive(&sm.ch, l) == 5u) {
          float t[12], a0 = 0.f, a1 = 0.f;
#pragma unroll
          for (int i = 0; i < 12; ++i) t[i] = sm.hs[i][l];
#pragma unroll
          for (int i = 0; i < 12; ++i) {
            const bool first = 3 * (i >> 2) + ((i & 3) + 1) / 2 < n0;
            a0 += first ? t[i] : 0.f; a1 += first ? 0.f : t[i];
          }
          const u32x2 r0 = {__float_as_uint(a0), tag}, r1 = {__float_as_uint(a1), tag};
          __builtin_amdgcn_raw_buffer_store_b64(r0, rWS, hA, 0, AUX_SC1);
          __builtin_amdgcn_raw_buffer_store_b64(r1, rWS, hB, 0, AUX_SC1);
          STAMP(6);
        }
        FENCE();
      }
    } else {
#define TOUCH 1
#define AUX_T 0
      u32 tc0, tc1 = 0u;
      if (w == 6) {
        const rsrc_t rV = mkr(Vc, 33554432u);
        constexpr u32 TG = TOUCH == 1 ? 65536u : TOUCH == 2 ? 16384u : 4096u;
        tc0 = __builtin_amdgcn_raw_buffer_load_b32(rV, l < (int)(131072u / TG) ? (u32)u * 131072u + (u32)l * TG : OOR, 0, AUX_T);
      } else {
        constexpr int TN = TOUCH == 3 ? 6 : 2;
        tc0 = __builtin_amdgcn_raw_buffer_load_b32(rD, l < TN ? (u32)(18 * u) * 1152u + (u32)l * (20608u / (TN - 1)) / 128u * 128u : OOR, 0, AUX_T);
        tc1 = __builtin_amdgcn_raw_buffer_load_b32(rO, l < TN ? (u32)(16 * u) * 1152u + (u32)l * (18304u / (TN - 1)) / 128u * 128u : OOR, 0, AUX_T);
      }
      FENCE();
      if (w == 6) {
        const unsigned char* fp = ws + WS_FL + (u32)u * 128u;
        LWAIT(cq, 8u);
        for (u32 it = 0; it < SPIN_G; ++it) {
          u32x16 v;
          __builtin_amdgcn_s_sleep(QSLP);
          asm volatile("s_load_dwordx16 %0, %1, 0x0 glc\n s_waitcnt lgkmcnt(0)" : "=&s"(v) : "s"(fp) : "memory");
          u32 bad = 0;
#pragma unroll
          for (int e = 0; e < 16; ++e) bad |= v[e] ^ tag;
          if (bad == 0) break;
        }
        if (l == 0) lds_st(&sm.ff, 1u);
      } else {
        LWAIT(ff, 1u);
      }
      if (w == 6) STAMP(4);
      const u32 g1a = WS_QX + (u32)(g * 16 + (w - 6) * 8 + (l >> 4)) * 512u + (l & 15) * 16;
      u32x4 g1[4];
#pragma unroll
      for (int i = 0; i < 4; ++i) g1[i] = __builtin_amdgcn_raw_buffer_load_b128(rWS, g1a + (i & 1) * 256 + (i >> 1) * 2048, 0, AUX_1ST);
      asm volatile("" : "+v"(g1[0]), "+v"(g1[1]), "+v"(g1[2]), "+v"(g1[3]));
      asm volatile("" :: "v"(tc0), "v"(tc1));
      if (w == 6) STAMP(16);
      if (!__all(tagok(g1[0], tag) && tagok(g1[1], tag) && tagok(g1[2], tag) && tagok(g1[3], tag))) {
        for (u32 it = 0;; ++it) {
          asm volatile("" ::: "memory");
#pragma unroll
          for (int i = 0; i < 4; ++i) g1[i] = __builtin_amdgcn_raw_buffer_load_b128(rWS, g1a + (i & 1) * 256 + (i >> 1) * 2048, 0, AUX_SC1);
          asm volatile("" : "+v"(g1[0]), "+v"(g1[1]), "+v"(g1[2]), "+v"(g1[3]));
          if (__all(tagok(g1[0], tag) && tagok(g1[1], tag) && tagok(g1[2], tag) && tagok(g1[3], tag)) || it >= SPIN_G) break;
        }
      }
#pragma unroll
      for (int i = 0; i < 4; ++i) {
        float a = red16(__uint_as_float(g1[i][0]), __uint_as_float(g1[i][2]));
        a = red32(a, a);
        const u32 hib = __builtin_bit_cast(unsigned short, (__bf16)a);
        *((lds_u16*)&sm.qv[w - 6][l >> 5][16 * i + (l & 15)] + ((l >> 4) & 1)) = (unsigned short)(l < 32 ? hib : 0u);
      }
      LDSWAIT();
      BUMP(fq);
      if (w == 6) STAMP(5);
      WST(3);
    }
    FENCE();
    if (w == 7) {
      {
        const u32 ab = l < 18 ? WS_AG + (u32)g * 512u + l * 16 : OOR;
        u32x4 e0;
        for (u32 it = 0;; ++it) {
          asm volatile("" ::: "memory");
          e0 = __builtin_amdgcn_raw_buffer_load_b128(rWS, ab, 0, AUX_SC1);
          if (__all(l >= 18 || tagok(e0, tag)) || it >= SPIN_G) break;
          __builtin_amdgcn_s_sleep(8);
        }
        const int d0 = min(max((int)(e0[0] - tb), -200), 200), d1 = min(max((int)(e0[2] - tb), -200), 200);
        const float dm = rowsum16((float)(d0 + d1)) * (1.0f / 32.0f);
        if (l == 0) lds_st(&sm.an, (tb + (u32)((int)(dm + 256.5f) - 256 + AOFF)) | 1u);
        STAMP(19);
      }
      const bool on = (l & 15) < 9 && l < 48;
      const u32 hb = on ? WS_HX + (u32)u * 1024u + (u32)(l >> 4) * 144u + (l & 15) * 16 : OOR;
      const unsigned char* hs = ws + WS_HX + (u32)u * 1024u;
      LWAIT(ch, 6u);
      for (u32 it = 0; it < SPIN_G; ++it) {
        u32 a0, a6; u32x4 a1, a2, a3, a4, a5;
        __builtin_amdgcn_s_sleep(HSLP);
        asm volatile("s_load_dword %0, %7, 0x4 glc\n s_load_dwordx4 %1, %7, 0x8c glc\n s_load_dwordx4 %2, %7, 0x11c glc\n s_load_dwordx4 %3, %7, 0x1ac glc\n"
                     " s_load_dwordx4 %4, %7, 0x23c glc\n s_load_dwordx4 %5, %7, 0x2cc glc\n s_load_dword %6, %7, 0x35c glc\n s_waitcnt lgkmcnt(0)"
                     : "=&s"(a0), "=&s"(a1), "=&s"(a2), "=&s"(a3), "=&s"(a4), "=&s"(a5), "=&s"(a6) : "s"(hs) : "memory");
        if (((a0 ^ tag) | (a6 ^ tag) | (a1[0] ^ tag) | (a1[2] ^ tag) | (a2[0] ^ tag) | (a2[2] ^ tag) | (a3[0] ^ tag) | (a3[2] ^ tag) |
             (a4[0] ^ tag) | (a4[2] ^ tag) | (a5[0] ^ tag) | (a5[2] ^ tag)) == 0) break;
      }
      u32x4 t0, t1;
      for (u32 it = 0;; ++it) {
        asm volatile("" ::: "memory");
        t0 = __builtin_amdgcn_raw_buffer_load_b128(rWS, hb, 0, AUX_SC1);
        t1 = __builtin_amdgcn_raw_buffer_load_b128(rWS, hb + 432, 0, AUX_SC1);
        if (__all(!on || (tagok(t0, tag) && tagok(t1, tag))) || it >= SPIN_G) break;
        __builtin_amdgcn_s_sleep(8);
      }
      float gs = red16(__uint_as_float(t0[0]), __uint_as_float(t0[2])), us = red16(__uint_as_float(t1[0]), __uint_as_float(t1[2]));
      gs = red32(gs, gs); us = red32(us, us);
      const float hval = on ? gs / (1.0f + __expf(-gs)) * us : 0.f;
      if (l < 32 && (l & 15) < 12) lds_st(&sm.hv[2 * (l & 15) + (l >> 4)], nosent(hval));
      STAMP(10);
    } else if (w == 6) {
      LWAIT(cm, 8u);
      const int h = (l >> 3) & 1;
      const float mi = sm.wmax[h][l & 7], li = sm.wl[h][l & 7];
      float M = fmaxf(mi, dpp<0xB1>(mi)); M = fmaxf(M, dpp<0x4E>(M)); M = fmaxf(M, dpp<0x141>(M));
      float L = li * __builtin_amdgcn_exp2f(mi - M);
      L += dpp<0xB1>(L); L += dpp<0x4E>(L); L += dpp<0x141>(L);
      const u32x2 ml = {nosent(M), nosent(L)};
      __builtin_amdgcn_raw_buffer_store_b64(ml, rWS, l < 16 ? WS_ML + (u32)(g * 16 + l) * 128u + j * 8 : OOR, 0, AUX_SC1);
      STAMP(17);
      __builtin_amdgcn_s_sleep(MSLP0);
      const unsigned char* mp = ws + WS_ML + (u32)u * 128u;
      u32x16 v[2];
      for (u32 it = 0; it < SPIN_G; ++it) {
        asm volatile("s_load_dwordx16 %0, %1, 0x0 glc" : "=&s"(v[0]) : "s"(mp) : "memory");
        asm volatile("s_load_dwordx16 %0, %1, 0x40 glc" : "=&s"(v[1]) : "s"(mp) : "memory");
        asm volatile("s_waitcnt lgkmcnt(0)" ::: "memory");
        u32 mx = 0;
#pragma unroll
        for (int k = 0; k < 2; ++k) {
          asm volatile("" : "+s"(v[k]));
#pragma unroll
          for (int e = 0; e < 16; ++e) mx = mx > v[k][e] ? mx : v[k][e];
        }
        if (mx != SENT) break;
        __builtin_amdgcn_s_sleep(MSLP);
      }
      u32 tm = 0, tl = 0;
#pragma unroll
      for (int k = 0; k < 2; ++k)
#pragma unroll
        for (int e = 0; e < 8; ++e) {
          asm("v_writelane_b32 %0, %1, %2" : "+v"(tm) : "s"(v[k][2 * e]), "n"(8 * k + e));
          asm("v_writelane_b32 %0, %1, %2" : "+v"(tl) : "s"(v[k][2 * e + 1]), "n"(8 * k + e));
        }
      asm volatile("s_nop 1" : "+v"(tm), "+v"(tl));
      const float mi2 = __uint_as_float(tm);
      const float e = __builtin_amdgcn_exp2f(mi2 - rowmax16(mi2));
      const float L2 = rowsum16(e * __uint_as_float(tl));
      if (l < 16) lds_st(&sm.wgt[l], nosent(e / L2));
      STAMP(18);
    }
    FENCE();
    for (u32 it = 0; lds_ld(&sm.cv) < TRIGN && it < SPIN_L; ++it) __builtin_amdgcn_s_sleep(1);
    asm volatile("" ::: "memory");
    if (w == 0) STAMP(24);
    DWADDR();
    u32 td0;
    ANCHOR(td0);
    td0 += (u32)(w * (DSTAG / 10));
#define DPACE(k) PACE_AT(td0, (TK(31) + DOFF + (k) * DTAU) / 10)
    DPACE(0); dw[0] = __builtin_amdgcn_raw_buffer_load_b128(rD, od, 0, AUX_NT); FENCE();
    DPACE(1); dw[1] = __builtin_amdgcn_raw_buffer_load_b128(rD, od, 8 * 1152, AUX_NT); FENCE();
    DPACE(2); dw[3] = __builtin_amdgcn_raw_buffer_load_b128(rO, oo, 0, AUX_NT); FENCE();
    DPACE(3); dw[4] = __builtin_amdgcn_raw_buffer_load_b128(rO, oo, 8 * 1152, AUX_NT); FENCE();
    dw[2] = __builtin_amdgcn_raw_buffer_load_b128(rD, l < 16 ? od : OOR, 16 * 1152, AUX_NT); FENCE();
    if (w == 0) STAMP(7);
    FENCE();
  }
  FENCE();
  if (W8P == 16 && w == 8) { DWADDR(); DWLOADS(); }
  FENCE();
  float y[8];
  if (ow) {
    const int rs = l >> 3;
    u32 h0, h1, h2;
    for (u32 it = 0;; ++it) {
      h0 = lds_ld(&sm.hv[rs]); h1 = lds_ld(&sm.hv[8 + rs]); h2 = lds_ld(&sm.hv[16 + rs]);
      if (__all(h0 != SENT && h1 != SENT && h2 != SENT) || it >= SPIN_L) break;
      __builtin_amdgcn_s_sleep(1);
    }
#pragma unroll
    for (int e = 0; e < 4; ++e) {
      y[2 * e] = __uint_as_float(h0) * bflo(dw[0][e]) + __uint_as_float(h1) * bflo(dw[1][e]) + __uint_as_float(h2) * bflo(dw[2][e]);
      y[2 * e + 1] = __uint_as_float(h0) * bfhi(dw[0][e]) + __uint_as_float(h1) * bfhi(dw[1][e]) + __uint_as_float(h2) * bfhi(dw[2][e]);
    }
    if (w == 0) STAMP(13);
  }
  FENCE();
  u32 a1 = 0u, fxo = ow ? WS_FX + (u32)(4 * w + ((l >> 1) & 3)) * 16384u + (u32)u * 64u +
                          (8 * (l & 1) + 4 * (l >> 5) + 2 * ((l >> 4) & 1) + ((l >> 3) & 1)) * 4 : OOR;
  asm volatile("" : "+v"(fxo));
  if (!ow) {
    const int p0 = w - 9;
    u32 wb_;
    for (u32 it = 0;; ++it) {
      wb_ = lds_ld(&sm.wgt[(p0 + 7 * (l >> 4)) & 15]);
      if (__all(wb_ != SENT) || it >= SPIN_L) break;
      __builtin_amdgcn_s_sleep(1);
    }
    const unsigned char* op = ws + WS_OX + (u32)u * (16 * OXS) + (u32)p0 * OXS;
    u32 pend = p0 < 2 ? 7u : 3u;
    for (u32 it = 0; it < SPIN_G && pend; ++it) {
      u32x16 v[3];
      if (GSL) __builtin_amdgcn_s_sleep(GSL);
      if (pend & 1u) asm volatile("s_load_dwordx16 %0, %1, 0x0 glc" : "=&s"(v[0]) : "s"(op) : "memory");
      if (pend & 2u) asm volatile("s_load_dwordx16 %0, %1, %2 glc" : "=&s"(v[1]) : "s"(op), "n"(7 * OXS) : "memory");
      if (pend & 4u) asm volatile("s_load_dwordx16 %0, %1, %2 glc" : "=&s"(v[2]) : "s"(op), "n"(14 * OXS) : "memory");
      asm volatile("s_waitcnt lgkmcnt(0)" ::: "memory");
#pragma unroll
      for (int k = 0; k < 3; ++k) {
        if (pend & (1u << k)) {
          asm volatile("" : "+s"(v[k]));
          u32 t;
#define WL(e) " v_writelane_b32 %0, %" #e ", %17+" #e "-1\n"
          asm("v_mov_b32 %0, 0\n" WL(1) WL(2) WL(3) WL(4) WL(5) WL(6) WL(7) WL(8) WL(9) WL(10) WL(11) WL(12) WL(13) WL(14) WL(15) WL(16)
              : "=&v"(t) : "s"(v[k][0]), "s"(v[k][1]), "s"(v[k][2]), "s"(v[k][3]), "s"(v[k][4]), "s"(v[k][5]), "s"(v[k][6]), "s"(v[k][7]),
                "s"(v[k][8]), "s"(v[k][9]), "s"(v[k][10]), "s"(v[k][11]), "s"(v[k][12]), "s"(v[k][13]), "s"(v[k][14]), "s"(v[k][15]), "n"(16 * k));
          if (__builtin_amdgcn_ballot_w64(t == SENT) == 0ull) {
            pend &= ~(1u << k);
            const u32 r = nosent(__uint_as_float(t) * __uint_as_float(wb_));
            const int p = p0 + 7 * k;
            if ((l >> 4) == k) lds_st(&sm.op[(((l >> 1) & 7) * 8 + (p >> 1)) * 4 + (l & 1) * 2 + (p & 1)], r);
          }
        }
      }
    }
    if (w == 11) STAMP(27);
    if (w == 15) STAMP(28);
    if (pend) { lds_st(&sm.op[4 * l], 0u); lds_st(&sm.op[4 * l + 1], 0u); lds_st(&sm.op[4 * l + 2], 0u); lds_st(&sm.op[4 * l + 3], 0u); }
    if (w == 9) STAMP(20);
    if (w == 13) STAMP(15);
  } else {
    f32x2 wf[8], y2[4];
#pragma unroll
    for (int e = 0; e < 4; ++e) { wf[e] = f32x2{bflo(dw[3][e]), bfhi(dw[3][e])}; wf[4 + e] = f32x2{bflo(dw[4][e]), bfhi(dw[4][e])}; y2[e] = f32x2{y[2 * e], y[2 * e + 1]}; }
    PIN8(wf);
    asm volatile("" : "+v"(y2[0]), "+v"(y2[1]), "+v"(y2[2]), "+v"(y2[3]));
    FENCE();
    if (w == 0) STAMP(21);
    __builtin_amdgcn_s_setprio(0);
#define OSN 1
#define OSL " s_sleep 1\n"
    u32x4 o4;
    {
      u32 tm, cnt = SPIN_L;
      const u32 oa = (u32)(size_t)(lds_u32*)&sm.op[4 * l];
      asm volatile("1: ds_read_b128 v[108:111], %[a]\n" OSL " s_sub_u32 %[c], %[c], 1\n s_cbranch_scc1 2f\n s_waitcnt lgkmcnt(0)\n"
                   " v_max3_u32 %[tm], v108, v109, v110\n v_max_u32 %[tm], %[tm], v111\n v_cmp_eq_u32 vcc, -1, %[tm]\n s_cbranch_vccnz 1b\n"
                   "2: s_waitcnt lgkmcnt(0)"
                   : "={v[108:111]}"(o4), [tm] "=&v"(tm), [c] "+s"(cnt) : [a] "v"(oa) : "vcc", "scc", "memory");
      FENCE();
    }
    float o0 = __uint_as_float(o4[0]) + __uint_as_float(o4[1]), o1 = __uint_as_float(o4[2]) + __uint_as_float(o4[3]);
    o0 += dpp<0xB1>(o0); o1 += dpp<0xB1>(o1);
    o0 += dpp<0x4E>(o0); o1 += dpp<0x4E>(o1);
    o0 += dpp<0x141>(o0); o1 += dpp<0x141>(o1);
#pragma unroll
    for (int e = 0; e < 4; ++e) y2[e] = __builtin_elementwise_fma(f32x2{o0, o0}, wf[e], y2[e]);
#pragma unroll
    for (int e = 0; e < 4; ++e) y2[e] = __builtin_elementwise_fma(f32x2{o1, o1}, wf[4 + e], y2[e]);
#pragma unroll
    for (int e = 0; e < 4; ++e) { y[2 * e] = y2[e][0]; y[2 * e + 1] = y2[e][1]; }
    float a4[4], a2[2];
#pragma unroll
    for (int e = 0; e < 4; ++e) a4[e] = red32(y[e], y[e + 4]);
#pragma unroll
    for (int e = 0; e < 2; ++e) a2[e] = red16(a4[e], a4[e + 2]);
    const float t_ = (l & 8) ? a2[0] : a2[1], kp_ = (l & 8) ? a2[1] : a2[0];
    a1 = nosent(kp_ + dpp<0x128>(t_));
  }
  __builtin_amdgcn_s_waitcnt(0x0F70);
  __builtin_amdgcn_raw_buffer_store_b32(a1, rWS, fxo, 0, AUX_SC1);
  if (w == 0) STAMP(14);
  if (fcons) {
    if (w >= 9) {
      for (u32 it = 0; it < SPIN_L; ++it) {
        const u32x4 o4 = lds_ld4(&sm.op[4 * l]);
        if (__all(o4[0] != SENT && o4[1] != SENT && o4[2] != SENT && o4[3] != SENT)) break;
        __builtin_amdgcn_s_sleep(2);
      }
    }
#define FDELAY 12
    __builtin_amdgcn_s_sleep(FDELAY);
    const int col = 4 * (l & 3) + 2 * (l >> 5) + ((l >> 4) & 1);
    float xr = bflo(*((lds_u16*)sm.xs + cidx * 16 + col));
    u32 fw = (u32)(size_t)(lds_u32*)&sm.fin[w][col], fr = (u32)(size_t)(lds_u32*)&sm.fin[0][4 * l], po = (l & 12) == 0 ? (cidx * 16 + col) * 2 : OOR;
    fw = (l & 12) == 0 ? fw : (u32)(size_t)(lds_u32*)(u32*)&sm.qp[0][l];
    const rsrc_t rY = mkr(out, 1152u);
    u32 e1 = sm.ev + 1u, eo = (cidx == 0 && l == 0) ? WS_E : OOR;
    asm volatile("" : "+v"(xr), "+v"(fw), "+v"(fr), "+v"(po), "+v"(e1), "+v"(eo) : "s"(rY));
    u32 fa = WS_FX + cidx * 16384u + (u32)w * 1024u + l * 16;
    u32x4 t = {0u, 0u, 0u, 0u};
#define FP 3
#define FPS 4
    u32x4 r0, r1, r2, r3;
    {
      unsigned long long sv;
      u32 tm, cnt = SPIN_G;
#define FLD(a, b) "buffer_load_dwordx4 v[" #a ":" #b "], %[fa], %[rs], 0 offen sc1\n"
#define FST(a, a1, a2, b) "s_waitcnt vmcnt(%[n])\n v_max3_u32 %[tm], v" #a ", v" #a1 ", v" #a2 "\n v_max_u32 %[tm], %[tm], v" #b "\n"       \
      " v_cmp_ne_u32 vcc, -1, %[tm]\n v_mov_b64 v[108:109], v[" #a ":" #a1 "]\n v_mov_b64 v[110:111], v[" #a2 ":" #b "]\n"                     \
      " s_andn2_b64 exec, exec, vcc\n s_cbranch_execz 2f\n" FLD(a, b)
      asm volatile("s_mov_b64 %[sv], exec\n"
                   FLD(112, 115) "s_sleep %[sp]\n" FLD(116, 119)
                   "s_sleep %[sp]\n" FLD(120, 123)
                   "1:\n" FST(112, 113, 114, 115) FST(116, 117, 118, 119)
                   FST(120, 121, 122, 123)
                   "s_sub_u32 %[c], %[c], 1\n s_cbranch_scc0 1b\n"
                   "2: s_mov_b64 exec, %[sv]\n"
                   : "={v[108:111]}"(t), "={v[112:115]}"(r0), "={v[116:119]}"(r1), "={v[120:123]}"(r2), "={v[124:127]}"(r3),
                     [sv] "=&s"(sv), [tm] "=&v"(tm), [c] "+s"(cnt)
                   : [fa] "v"(fa), [rs] "s"(rWS), [n] "n"(FP - 1), [sp] "n"(FPS) : "vcc", "scc", "memory");
      FENCE();
    }
#define FRED(t) ({ float c_ = red16(red32(__uint_as_float(t[0]), __uint_as_float(t[2])), red32(__uint_as_float(t[1]), __uint_as_float(t[3]))); \
      c_ += dpp<0x124>(c_); c_ + dpp<0x128>(c_); })
    u32x4 f0;
    {
      const u32 c = nosent(FRED(t));
      asm volatile("ds_write_b32 %[fw], %[v]\n ds_read_b128 %[f], %[a]\n s_waitcnt lgkmcnt(0)"
                   : [f] "=&v"(f0) : [a] "v"(fr), [fw] "v"(fw), [v] "v"(c) : "memory");
      FENCE();
    }
    if (__builtin_amdgcn_ballot_w64(max(max(f0[0], f0[1]), max(f0[2], f0[3])) == SENT) == 0ull) {
      const float c = FRED(f0);
      const __bf16 r = (__bf16)(xr + c);
      __builtin_amdgcn_raw_buffer_store_b16(__builtin_bit_cast(unsigned short, r), rY, po, 0, 0);
      __builtin_amdgcn_raw_buffer_store_b32(e1, rWS, eo, 0, AUX_SC1);
      STAMP(23);
    }
    asm volatile("" :: "{v[112:115]}"(r0), "{v[116:119]}"(r1), "{v[120:123]}"(r2), "{v[124:127]}"(r3));
  }
}

extern "C" void kernel_launch(void* const* d_in, const int* in_sizes, int n_in, void* d_out, int out_size, void* d_ws, size_t ws_size,
                              hipStream_t stream) {
  fused_decode<<<256, 1024, 0, stream>>>((const unsigned short*)d_in[0], (const unsigned short*)d_in[1], (const unsigned short*)d_in[2],
                                         (const unsigned short*)d_in[3], (const unsigned short*)d_in[4], (const unsigned short*)d_in[6],
                                         (const unsigned short*)d_in[7], (const unsigned short*)d_in[8], (unsigned char*)d_ws,
                                         (unsigned short*)d_out);
}
```
